```python
import torch

def _softmax(x, axis=-1):
    return torch.softmax(x, dim=axis)
def _gelu(x):
    return torch.nn.functional.gelu(x, approximate="tanh")
def _silu(x):
    return torch.nn.functional.silu(x)
def _sigmoid(x):
    return torch.sigmoid(x)
def _layernorm(x, axis=-1, eps=1e-5):
    m = x.mean(dim=axis, keepdim=True)
    v = x.var(dim=axis, keepdim=True, unbiased=False)
    return (x - m) / torch.sqrt(v + eps)
def _scatter_add(tgt, idx, src, axis=0):
    return tgt.clone().index_add_(axis, idx, src)
def _segment_sum(src, seg, n):
    out = torch.zeros((n,) + src.shape[1:], dtype=src.dtype)
    return out.index_add_(0, seg, src)
def _pad(x, pw):
    flat = tuple(v for pair in reversed(pw) for v in pair)
    return torch.nn.functional.pad(x, flat)

kernel_name = 'concat_heads__softmax_full_mha__spectral_gfnet__prompt'

def setup_inputs():

    _w = (lambda a: (lambda t: t.to(torch.bfloat16) if t.is_floating_point() else t)(torch.as_tensor(a)))
    def _A_setup():
        B, S, D = 1, 2048, 448
        return {
            "x": _w((lambda *s: torch.randn(*s, dtype=torch.bfloat16) * 0.2)(B, S, D)),
            "wqkv": _w((lambda *s: torch.randn(*s, dtype=torch.bfloat16) * 0.2)(D, 3 * D)),
            "wo": _w((lambda *s: torch.randn(*s, dtype=torch.bfloat16) * 0.05)(D, D)),
        }
    _A_in = _A_setup()

    def _B_setup():
        B, S, D = 1, 2048, 448
        Df = D // 2 + 1
        return {
            "x": _w((lambda *s: torch.randn(*s, dtype=torch.bfloat16) * 0.05)(B, S, D)),
            "w_r": _w((lambda *s: torch.randn(*s, dtype=torch.bfloat16) * 6.4)(S, Df) * 0.02),
            "w_i": _w((lambda *s: torch.randn(*s, dtype=torch.bfloat16) * 6.4)(S, Df) * 0.02),
        }
    _B_in = _B_setup()

    return {
        "A_x": _A_in["x"],
        "A_wqkv": _A_in["wqkv"],
        "A_wo": _A_in["wo"],
        "B_x": _B_in["x"],
        "B_w_r": _B_in["w_r"],
        "B_w_i": _B_in["w_i"],
    }

def reference(A_x, A_wqkv, A_wo, B_x, B_w_r, B_w_i):
    def _A_ref(x, wqkv, wo):
        B, S, D = x.shape
        H = 16; Dh = D // H
        qkv = (lambda x, *d: x.permute(*d))((x @ wqkv).reshape(B, S, 3, H, Dh), 2, 0, 3, 1, 4)
        q, k, v = qkv[0], qkv[1], qkv[2]
        scale = Dh ** -0.5
        scores = torch.einsum("bhqd,bhkd->bhqk", q, k) * scale
        p = _softmax(scores, axis=-1)
        o = torch.einsum("bhqk,bhkd->bhqd", p, v)
        return (lambda x, *d: x.permute(*d))(o, 0, 2, 1, 3).reshape(B, S, D) @ wo

    def _B_ref(x, w_r, w_i):
        B, S, D = x.shape
        X = (lambda x, axes=(-2,-1): torch.fft.rfft2(x.float(), dim=axes))(x, axes=(-2, -1))
        W = w_r + 1j * w_i
        X = X * W
        y = (lambda x, s, axes=(-2,-1): torch.fft.irfft2(x, s=s, dim=axes).to(torch.bfloat16))(X, (S, D), axes=(-2, -1))
        return y

    a = _A_ref(A_x, A_wqkv, A_wo)
    b = _B_ref(B_x, B_w_r, B_w_i)
    return torch.concatenate([a, b], axis=-1)
```

```cpp
#include <hip/hip_runtime.h>
#include <hip/hip_bf16.h>
#include <stdint.h>

namespace tb {
constexpr double PI = 3.14159265358979323846264338327950288;
constexpr double tsin(double x) { double x2 = x * x, t = x, s = x; for (int i = 1; i < 12; i++) { t = -t * x2 / ((2 * i) * (2 * i + 1)); s += t; } return s; }
constexpr double tcos(double x) { double x2 = x * x, t = 1, s = 1; for (int i = 1; i < 12; i++) { t = -t * x2 / ((2 * i - 1) * (2 * i)); s += t; } return s; }
constexpr double cosrev(int m, int N) {
  if (2 * m > N) m = N - m;
  bool neg = false; if (4 * m > N) { m = N / 2 - m; neg = true; }
  double r = (8 * m <= N) ? tcos(2 * PI * m / N) : tsin(2 * PI * (N - 4 * m) / (4.0 * N));
  return neg ? -r : r; }
constexpr uint16_t tobf(double v) { float f = (float)v; uint32_t u = __builtin_bit_cast(uint32_t, f); u += 0x7FFF + ((u >> 16) & 1); return (uint16_t)(u >> 16); }
template <int N> struct Base { uint16_t c[N]; };
template <int N> constexpr Base<N> mkbase(double sc) { Base<N> b{}; for (int m = 0; m < N; m++) b.c[m] = tobf(sc * cosrev(m, N)); return b; }
constexpr double GSC = 2.0 / (448.0 * 2048.0);
constexpr Base<448> c448 = mkbase<448>(1.0);
constexpr Base<448> g448 = mkbase<448>(GSC);
constexpr Base<2048> c2048 = mkbase<2048>(1.0);
struct T28 { uint16_t v[28 * 512]; };
struct T32 { uint16_t v[32 * 512]; };
struct T24 { uint16_t v[24 * 512]; };
constexpr T28 mkF1(int a) { T28 t{}; int o = 0;
  for (int p = 0; p < 2; p++) for (int par = 0; par < 2; par++) for (int ks = 0; ks < 7; ks++) for (int l = 0; l < 64; l++) {
    int f = 16 * a + (l & 15), d0 = 2 * (32 * ks + 8 * (l >> 4)) + par, ph = p ? (par ? 112 : 336) : 0;
    if (f == 0 && p == 1) { f = 112; ph = par ? 112 : 0; }
    int m = (f * d0 + ph) % 448;
    for (int j = 0; j < 8; j++) { t.v[o++] = c448.c[m]; m += 2 * f; if (m >= 448) m -= 448; } }
  return t; }
constexpr T28 mkG1(int vp) { T28 t{}; int o = 0;
  for (int v = 2 * vp; v < 2 * vp + 2; v++) for (int a = 0; a < 7; a++) for (int par = 0; par < 2; par++) for (int l = 0; l < 64; l++) {
    int d = 32 * v + 2 * (l & 15) + par, f0 = 16 * a + 4 * (l >> 4);
    int m = (f0 * d) % 448;
    for (int j = 0; j < 4; j++) { int mi = m + 112; if (mi >= 448) mi -= 448;
      t.v[o + j] = g448.c[m]; t.v[o + 4 + j] = g448.c[mi]; m += d; if (m >= 448) m -= 448; }
    if (f0 == 0) { t.v[o] = tobf(0.5 * GSC); t.v[o + 4] = g448.c[(112 * d + (par ? 112 : 0)) % 448]; }
    o += 8; }
  return t; }
constexpr T32 mkE1(int nb) { T32 t{}; int o = 0;
  for (int n2 = 8 * nb; n2 < 8 * nb + 8; n2++) for (int kt = 0; kt < 2; kt++) for (int p = 0; p < 2; p++) for (int l = 0; l < 64; l++) {
    int k1 = 16 * kt + (l & 15), b = k1 * (256 * (l >> 4) + n2) + (p ? 512 : 0), s = 64 * k1;
    for (int j = 0; j < 4; j++) { t.v[o + j] = c2048.c[(b + j * s) & 2047]; t.v[o + 4 + j] = c2048.c[(b + (16 + j) * s) & 2047]; }
    o += 8; }
  return t; }
constexpr T32 mkE2(int nb) { T32 t{}; int o = 0;
  for (int n2 = 8 * nb; n2 < 8 * nb + 8; n2++) for (int nt = 0; nt < 2; nt++) for (int p = 0; p < 2; p++) for (int l = 0; l < 64; l++) {
    int s = 64 * (16 * nt + (l & 15)) + n2, b = 4 * (l >> 4) * s + (p ? 1536 : 0);
    for (int j = 0; j < 4; j++) { t.v[o + j] = c2048.c[(b + j * s) & 2047]; t.v[o + 4 + j] = c2048.c[(b + (16 + j) * s) & 2047]; }
    o += 8; }
  return t; }
constexpr T24 mkF64() { T24 t{}; int o = 0;
  for (int tt = 0; tt < 4; tt++) for (int ks = 0; ks < 2; ks++) for (int p = 0; p < 3; p++) for (int l = 0; l < 64; l++) {
    int a = 16 * tt + (l & 15);
    for (int j = 0; j < 8; j++) { int b = 32 * ks + 16 * (j >> 2) + 4 * (l >> 4) + (j & 3); t.v[o++] = c2048.c[(32 * a * b + (p == 0 ? 0 : p == 1 ? 512 : 1536)) & 2047]; } }
  return t; }
template <int I> constexpr T28 F1c = mkF1(I);
template <int I> constexpr T28 G1c = mkG1(I);
template <int I> constexpr T32 E1c = mkE1(I);
template <int I> constexpr T32 E2c = mkE2(I);
constexpr T24 F64c = mkF64();
struct All { T28 f1[7]; T28 g1[7]; T32 e1[8]; T32 e2[8]; T24 f64; };
}
constexpr unsigned TB_F1 = 0, TB_G1 = 7 * 28672, TB_E1 = 14 * 28672, TB_E2 = TB_E1 + 262144, TB_F64 = TB_E2 + 262144, TB_BYTES = TB_F64 + 24576;
static_assert(sizeof(tb::All) == TB_BYTES, "table size");
#if defined(__HIP_DEVICE_COMPILE__)
#define T7(N) tb::N<0>, tb::N<1>, tb::N<2>, tb::N<3>, tb::N<4>, tb::N<5>, tb::N<6>
#define T8(N) tb::N<0>, tb::N<1>, tb::N<2>, tb::N<3>, tb::N<4>, tb::N<5>, tb::N<6>, tb::N<7>
static __device__ const tb::All g_tab __attribute__((aligned(4096))) = {{T7(F1c)}, {T7(G1c)}, {T8(E1c)}, {T8(E2c)}, tb::F64c};
#else
static __device__ const tb::All g_tab __attribute__((aligned(4096))) = {};
#endif

typedef short bf16x8 __attribute__((ext_vector_type(8)));
typedef short s16x4 __attribute__((ext_vector_type(4)));
typedef float f32x4 __attribute__((ext_vector_type(4)));
typedef float f32x2 __attribute__((ext_vector_type(2)));
typedef float f32x16 __attribute__((ext_vector_type(16)));
typedef unsigned u32x4 __attribute__((ext_vector_type(4)));
typedef unsigned u32x2 __attribute__((ext_vector_type(2)));
typedef unsigned u32x8 __attribute__((ext_vector_type(8)));
typedef unsigned u32x16 __attribute__((ext_vector_type(16)));
typedef __bf16 bf16x2_t __attribute__((ext_vector_type(2)));
typedef __amdgpu_buffer_rsrc_t rsrc_t;
typedef unsigned long long u64;
#define LAS __attribute__((address_space(3)))
#define GAS __attribute__((address_space(1)))
typedef LAS char* lds_p;
#define DI __device__ __forceinline__
#define MFMA32(a, b, c) __builtin_amdgcn_mfma_f32_32x32x16_bf16(a, b, c, 0, 0, 0)
#define MFMA16(a, b, c) __builtin_amdgcn_mfma_f32_16x16x32_bf16(a, b, c, 0, 0, 0)
#define SBAR() __builtin_amdgcn_sched_barrier(0)
constexpr int SC1 = 16;

constexpr int SEQ = 2048, DM = 448;
constexpr unsigned F_EP = 0, F_INIT = 256, F_CNT = 512, F_ST = 6144, F_B = 49152, F_Q = 81920;
constexpr unsigned WS_KV = 1u << 20, WS_O = 5u << 20, WS_T = 7u << 20, WS_U = 9u << 20, WS_END = 11u << 20;
constexpr unsigned OOB = 0x80000000u;
constexpr int LDS_BYTES = 160 * 1024;
constexpr int SLOT = 24576, LDS_QX = 5 * SLOT, LDS_WQ = LDS_QX + 8192;
constexpr int LDS_RED = 0, LDS_WO = 49152, LDS_XIMG = 49152, LDS_NYQ = 106496, LDS_MX = 110592, LDS_XCH = 114688, LDS_BIMG = 131072, LDS_CNT = LDS_BYTES - 128;

enum { C_RED = 0, C_QN = 1, C_MARK = 2, C_BBAR = 3, C_QDONE = 4, C_KVR = 5, C_PAIR = 6, C_NYQ = 8, C_FT = 9, C_KVD = 10, C_WO = 11, C_TD = 13, C_UD = 14, C_FU = 15,
       C_KN = 16, C_X1 = 18, C_X2 = 20, C_WQ = 22, C_TAG = 24 };

DI unsigned cvtpk(float lo, float hi) { f32x2 v = {lo, hi}; return __builtin_bit_cast(unsigned, __builtin_convertvector(v, bf16x2_t)); }
DI bf16x8 asfrag(u32x4 v) { return __builtin_bit_cast(bf16x8, v); }
DI u32x4 xor4(u32x4 v, unsigned m) { asm("" : "+v"(v)); v ^= m; asm("" : "+v"(v)); return v; }
DI u32x4 pk44(f32x4 a, f32x4 b) { return (u32x4){cvtpk(a[0], a[1]), cvtpk(a[2], a[3]), cvtpk(b[0], b[1]), cvtpk(b[2], b[3])}; }
#define PK8(a, s) (u32x4){cvtpk(a[8 * s], a[8 * s + 1]), cvtpk(a[8 * s + 2], a[8 * s + 3]), cvtpk(a[8 * s + 4], a[8 * s + 5]), cvtpk(a[8 * s + 6], a[8 * s + 7])}
DI rsrc_t mkrsrc(const void* p, unsigned bytes) { return __builtin_amdgcn_make_buffer_rsrc((void*)p, 0, bytes, 0x00020000); }
DI u32x4 ldsr128(lds_p p) { return *(LAS u32x4*)p; }
DI void ldsw128(lds_p p, u32x4 v) { *(LAS u32x4*)p = v; }
DI s16x4 ldstr(lds_p p) { return __builtin_amdgcn_ds_read_tr16_b64_v4i16((LAS s16x4*)p); }
DI bf16x8 cat(s16x4 a, s16x4 b) { return __builtin_shufflevector(a, b, 0, 1, 2, 3, 4, 5, 6, 7); }
#define LD128(r, v, s, aux) __builtin_amdgcn_raw_buffer_load_b128(r, v, s, aux)
template <int AUX> DI void st128(u32x4 d, rsrc_t r, unsigned v, unsigned s) { __builtin_amdgcn_raw_buffer_store_b128(d, r, v, s, AUX); asm volatile("s_nop 1" ::"v"(d)); }
#define ST128(d, r, v, s, aux) st128<aux>(d, r, v, s)
DI void dma16(unsigned ldsaddr, unsigned voff, rsrc_t r, unsigned soff) {
  asm volatile("s_mov_b32 m0, %0\n\ts_nop 0\n\tbuffer_load_dwordx4 %1, %2, %3 offen lds" ::"s"(ldsaddr), "v"(voff), "s"(r), "s"(soff) : "memory"); }
DI void aload(u32x4& d, unsigned voff, rsrc_t r, unsigned soff) {
  asm volatile("buffer_load_dwordx4 %0, %1, %2, %3 offen" : "=v"(d) : "v"(voff), "s"(r), "s"(soff) : "memory"); }
#define WAITV(n) asm volatile("s_waitcnt vmcnt(" #n ")" ::: "memory")
#define BAR_V(n) asm volatile("s_waitcnt vmcnt(" #n ")\n\ts_barrier" ::: "memory")
#define BAR_L() asm volatile("s_waitcnt lgkmcnt(0)\n\ts_barrier" ::: "memory")
#define PINV(x) asm volatile("" : "+v"(x))
DI void lds_arrive(int c) {
  u64 sv;
  asm volatile("s_waitcnt lgkmcnt(0)\n\ts_mov_b64 %0, exec\n\ts_mov_b64 exec, 1\n\tds_add_u32 %1, %2\n\ts_mov_b64 exec, %0" : "=&s"(sv) : "v"(LDS_CNT + 4 * c), "v"(1u) : "memory");
}
template <int SLEEP = 1> DI void lds_wait(int c, unsigned target) {
  unsigned tv, ts;
  asm volatile("LW_%=:\n\tds_read_b32 %0, %2\n\ts_waitcnt lgkmcnt(0)\n\tv_readfirstlane_b32 %1, %0\n\ts_nop 0\n\ts_cmp_ge_u32 %1, %3\n\ts_cbranch_scc1 LE_%=\n\ts_sleep %4\n\ts_branch LW_%=\n\tLE_%=:"
               : "=&v"(tv), "=&s"(ts) : "v"(LDS_CNT + 4 * c), "s"(target), "i"(SLEEP) : "scc", "memory");
}
DI void role_bar(int c, unsigned target) { lds_arrive(c); lds_wait(c, target); }
DI int fresh_lane() { int l; asm volatile("v_mbcnt_lo_u32_b32 %0, -1, 0\n\tv_mbcnt_hi_u32_b32 %0, -1, %0" : "=v"(l)); return l; }
DI bool drain_last(int c, unsigned n) {
  u64 sv; unsigned old;
  asm volatile("s_waitcnt vmcnt(0) lgkmcnt(0)\n\ts_mov_b64 %0, exec\n\ts_mov_b64 exec, 1\n\tds_add_rtn_u32 %1, %2, %3\n\ts_mov_b64 exec, %0\n\ts_waitcnt lgkmcnt(0)"
               : "=&s"(sv), "=&v"(old) : "v"(LDS_CNT + 4 * c), "v"(1u) : "memory");
  return __builtin_amdgcn_readfirstlane(old) == n - 1;
}
DI void set_flag(char* ws, unsigned off, u64 tag) { __hip_atomic_store((GAS u64*)(ws + off), tag, __ATOMIC_RELAXED, __HIP_MEMORY_SCOPE_AGENT); }
constexpr int NREP = 64;
DI void count_and_tell(char* ws, int c, unsigned n, u64 tag, int lane) {
  u64 old = 0;
  if (lane == 0) old = __hip_atomic_fetch_add((GAS u64*)(ws + F_CNT + c * 256), 1ull, __ATOMIC_RELAXED, __HIP_MEMORY_SCOPE_AGENT);
  if (__builtin_amdgcn_readfirstlane((unsigned)(old - tag)) == n - 1) {
    set_flag(ws, F_B + lane * 128 + c * 8, tag);
  }
}
template <int SLEEP> DI void wait_flags(char* ws, unsigned off, u64 tag) {
#pragma unroll 1
  for (int spin = 0; spin < (1 << 17); ++spin) {
    const u64 v = __hip_atomic_load((GAS u64*)(ws + off), __ATOMIC_RELAXED, __HIP_MEMORY_SCOPE_AGENT);
    if (__all(v == tag)) break;
    __builtin_amdgcn_s_sleep(SLEEP);
  }
  asm volatile("" ::: "memory");
}
DI u64 mktag(u64 e) { return e * 6364136223846793005ull + 1442695040888963407ull; }
struct Params { const void* xa; const void* wqkv; const void* wo; const void* xb; const void* wr; const void* wi; void* out; char* ws; };

DI int wg_h(int x, int r) { return 4 * (x & 3) + (r & 3); }
DI int wg_t(int x, int r) { return 8 * (x >> 2) + (r >> 2); }
DI int wg_g(int x, int r) { return 2 * (x & 3) + (r & 1) + 8 * ((r >> 1) & 1); }
DI int wg_c0(int r) { return (2 * (r & 3) + (r >> 2)) % 7; }
constexpr int SLP = 4, GSL = 3;
constexpr float THR2 = 60.f * 60.f / (1.02f * 1.02f);

DI void slow_attention(lds_p lds, rsrc_t rkv, rsrc_t rw, unsigned oo, int w, int lane) {
  bf16x8 qf[4][2];
#pragma unroll
  for (int i = 0; i < 4; ++i)
#pragma unroll
    for (int s = 0; s < 2; ++s) qf[i][s] = asfrag(ldsr128(lds + LDS_QX + ((w + 1 + i) & 3) * 2048 + s * 1024 + lane * 16));
  float m[4] = {-INFINITY, -INFINITY, -INFINITY, -INFINITY};
#pragma unroll 1
  for (int j = 0; j < 16; ++j) {
    const bf16x8 k0 = asfrag(LD128(rkv, lane * 16, j * 4096, SC1)), k1 = asfrag(LD128(rkv, lane * 16 + 1024, j * 4096, SC1));
#pragma unroll
    for (int i = 0; i < 4; ++i) { f32x16 s = {}; s = MFMA32(k0, qf[i][0], s); s = MFMA32(k1, qf[i][1], s);
#pragma unroll
      for (int r = 0; r < 16; ++r) m[i] = fmaxf(m[i], s[r]); }
  }
  LAS float* mx = (LAS float*)(lds + LDS_MX);
#pragma unroll
  for (int i = 0; i < 4; ++i) mx[(((w + 1 + i) & 3) * 4 + w) * 64 + lane] = m[i];
  role_bar(C_RED, 8);
#pragma unroll
  for (int i = 0; i < 4; ++i) { float a = -INFINITY;
#pragma unroll
    for (int u = 0; u < 8; ++u) a = fmaxf(a, mx[(((w + 1 + i) & 3) * 4) * 64 + u * 32 + (lane & 31)]);
    m[i] = a; }
  f32x16 o[4] = {};
#pragma unroll 1
  for (int j = 0; j < 16; ++j) {
    bf16x8 kv[4];
#pragma unroll
    for (int u = 0; u < 4; ++u) kv[u] = asfrag(LD128(rkv, lane * 16 + u * 1024, j * 4096, SC1));
#pragma unroll
    for (int i = 0; i < 4; ++i) { f32x16 s = {}; s = MFMA32(kv[0], qf[i][0], s); s = MFMA32(kv[1], qf[i][1], s);
#pragma unroll
      for (int r = 0; r < 16; ++r) s[r] = __builtin_amdgcn_exp2f(s[r] - m[i]);
      o[i] = MFMA32(kv[2], asfrag(PK8(s, 0)), o[i]); o[i] = MFMA32(kv[3], asfrag(PK8(s, 1)), o[i]); }
  }
#pragma unroll
  for (int i = 0; i < 3; ++i)
#pragma unroll
    for (int q = 0; q < 4; ++q) *(LAS f32x4*)(lds + LDS_RED + ((((w + 1 + i) & 3) * 3 + i) * 4 + q) * 1024 + lane * 16) = (f32x4){o[i][4 * q], o[i][4 * q + 1], o[i][4 * q + 2], o[i][4 * q + 3]};
  role_bar(C_RED, 12);
  f32x16 O = o[3];
#pragma unroll
  for (int i = 0; i < 3; ++i)
#pragma unroll
    for (int q = 0; q < 4; ++q) { const f32x4 a = *(LAS f32x4*)(lds + LDS_RED + ((w * 3 + i) * 4 + q) * 1024 + lane * 16);
#pragma unroll
      for (int e = 0; e < 4; ++e) O[4 * q + e] += a[e]; }
  auto sw = __builtin_amdgcn_permlane32_swap(__float_as_uint(O[12]), __float_as_uint(O[12]), false, false);
  O *= __builtin_amdgcn_rcpf(__uint_as_float(sw[1]));
  u32x4 f1 = PK8(O, 1); f1[2] = lane < 32 ? f1[2] : 0u; f1[3] = lane < 32 ? f1[3] : 0u;
  u32x4 f0 = PK8(O, 0); f0[1] = min(f0[1], 0xfffffffeu); f0[3] = min(f0[3], 0xfffffffeu); f1[1] = min(f1[1], 0xfffffffeu); f1[3] = min(f1[3], 0xfffffffeu);
  ST128(f0, rw, lane * 16, oo, SC1); ST128(f1, rw, lane * 16 + 1024, oo, SC1);
}

DI void role_a(const Params& p, lds_p lds, int x, int r, int w, int lane) {
  const int h = wg_h(x, r), t = wg_t(x, r);
  const int r32 = lane & 31, hh = lane >> 5, q = (lane >> 2) & 3, m = 4 * ((lane >> 4) & 1) + (lane & 3);
  const int row = 32 * w + r32, sw = (row >> 1) & 7, isv = wg_g(x, r) >> 3, hd = (2 * wg_g(x, r)) & 15;
  u32x16 va, vb;
#pragma unroll
  for (int ks = 0; ks < 4; ++ks) { va[ks] = row * 128 + (((2 * ks + hh) ^ sw) << 4); vb[1 + ks] = LDS_QX + ((w + 1 + ks) & 3) * 2048 + lane * 16; }
  va[4] = 16384 + (8 * hh + q) * 128 + (((m >> 1) ^ ((q >> 1) << 2)) << 4) + (m & 1) * 8;
  va[5] = 16384 + (8 * hh + q) * 128 + ((((m + 7) >> 1) ^ ((q >> 1) << 2)) << 4) + ((m + 7) & 1) * 8;
  va[6] = LDS_WQ + (8 * hh + q) * 64 + m * 8;
  va[7] = lane * 16;
  va[8] = (F_Q + (hd * 4 + lane) * 256 + (16 * isv + t) * 8) | ((7 - lane) & OOB); va[9] = F_Q + (h * 4 + (t >> 2)) * 256 + r32 * 8;
#pragma unroll
  for (int i = 0; i < 3; ++i) va[10 + i] = LDS_RED + (((w + 1 + i) & 3) * 3 + i) * 4096 + lane * 16;
  va[13] = LDS_RED + w * 12288 + lane * 16;
  va[14] = F_ST + (h * 32 + r32) * 8;
  va[15] = (F_ST + ((hd + lane) * 32 + 16 * isv + t) * 8) | ((1 - lane) & OOB);
  vb[0] = LDS_QX + w * 2048 + lane * 16;
  vb[5] = r32 == 28 ? 0x3f803f80u : 0u; vb[6] = LDS_CNT; vb[7] = 1; vb[8] = 0; vb[9] = 0; vb[10] = 0; vb[11] = 0; vb[12] = 0; vb[13] = 0;
  vb[14] = __float_as_uint((lane & 16) ? 0x1p40f : THR2); vb[15] = 0;
  rsrc_t rw = mkrsrc(p.ws, WS_END), rkv = mkrsrc(p.ws + WS_KV + h * 262144 + w * 65536, 65536);
  const unsigned d0 = WS_KV + (hd * 64 + 4 * t + w) * 4096 + isv * 2048;
  const unsigned oo = WS_O + ((4 * t + w) * 16 + h) * 2048;
  const int ir = w & 1, ik = w >> 1, u = 2 * (4 * x + r / 7) + ir;
  u32x8 ta;
  {
    const int cb = (16 * (m >> 2) + 8 * (m & 1) + 4 * ((m >> 1) & 1)) * 2;
    ta[0] = lane * 16;
    ta[1] = LDS_WO + ik * 28672 + (4 * hh + q) * 128 + cb + (((q >> 1) ^ ik) << 6);
    ta[2] = ta[1] - hh * 512;
    ta[3] = LDS_XCH + w * 4096 + lane * 16;
    ta[4] = (32 * u + r32) * 1792 + hh * 16; ta[5] = LDS_CNT + 4 * (C_PAIR + ir); ta[6] = 1; ta[7] = 0; }
  rsrc_t rout = mkrsrc(p.out, SEQ * 896 * 2);
  unsigned ol = WS_O + (u * 16 + 8 * ik) * 2048, oc = (r % 7) * 128 + ik * 64, rr = r;
  unsigned bad;
  asm volatile(
  "s_mov_b32 s58, 0x0fffffff\n\t"
  "s_mov_b32 s59, 0x0fffffff\n\t"
  "s_mov_b32 s60, -1\n\t"
  "s_mov_b32 s61, 0\n\t"
  "s_waitcnt lgkmcnt(0)\n\t"
  "s_cmp_lg_u32 s50, 0\n\t"
  "s_cbranch_scc1 LV_%=\n\t"
  "s_barrier\n\t"
  "ds_read_b128 v[144:147], v32\n\t"
  "ds_read_b64_tr_b16 v[64:65], v36 offset:0\n\t"
  "ds_read_b64_tr_b16 v[66:67], v36 offset:512\n\t"
  "ds_read_b64_tr_b16 v[68:69], v37 offset:0\n\t"
  "ds_read_b64_tr_b16 v[70:71], v37 offset:512\n\t"
  "ds_read_b128 v[148:151], v33\n\t"
  "ds_read_b64_tr_b16 v[72:73], v36 offset:2048\n\t"
  "ds_read_b64_tr_b16 v[74:75], v36 offset:2560\n\t"
  "ds_read_b64_tr_b16 v[76:77], v37 offset:2048\n\t"
  "ds_read_b64_tr_b16 v[78:79], v37 offset:2560\n\t"
  "s_waitcnt lgkmcnt(5)\n\t"
  "v_mfma_f32_32x32x16_bf16 v[96:111], v[64:67], v[144:147], 0\n\t"
  "ds_read_b128 v[152:155], v34\n\t"
  "ds_read_b64_tr_b16 v[80:81], v36 offset:4096\n\t"
  "ds_read_b64_tr_b16 v[82:83], v36 offset:4608\n\t"
  "v_mfma_f32_32x32x16_bf16 v[112:127], v[68:71], v[144:147], 0\n\t"
  "ds_read_b64_tr_b16 v[84:85], v37 offset:4096\n\t"
  "ds_read_b64_tr_b16 v[86:87], v37 offset:4608\n\t"
  "v_add_u32 v32, 0x6000, v32\n\t"
  "v_add_u32 v34, 0x6000, v34\n\t"
  "s_waitcnt lgkmcnt(5)\n\t"
  "v_mfma_f32_32x32x16_bf16 v[96:111], v[72:75], v[148:151], v[96:111]\n\t"
  "ds_read_b128 v[156:159], v35\n\t"
  "ds_read_b64_tr_b16 v[88:89], v36 offset:6144\n\t"
  "ds_read_b64_tr_b16 v[90:91], v36 offset:6656\n\t"
  "v_mfma_f32_32x32x16_bf16 v[112:127], v[76:79], v[148:151], v[112:127]\n\t"
  "ds_read_b64_tr_b16 v[92:93], v37 offset:6144\n\t"
  "ds_read_b64_tr_b16 v[94:95], v37 offset:6656\n\t"
  "v_add_u32 v33, 0x6000, v33\n\t"
  "v_add_u32 v35, 0x6000, v35\n\t"
  "v_add_u32 v36, 0x6000, v36\n\t"
  "v_add_u32 v37, 0x6000, v37\n\t"
  "s_waitcnt lgkmcnt(5)\n\t"
  "v_mfma_f32_32x32x16_bf16 v[96:111], v[80:83], v[152:155], v[96:111]\n\t"
  "v_mfma_f32_32x32x16_bf16 v[112:127], v[84:87], v[152:155], v[112:127]\n\t"
  "s_barrier\n\t"
  "ds_read_b128 v[160:163], v32\n\t"
  "ds_read_b64_tr_b16 v[64:65], v36 offset:0\n\t"
  "ds_read_b64_tr_b16 v[66:67], v36 offset:512\n\t"
  "ds_read_b64_tr_b16 v[68:69], v37 offset:0\n\t"
  "ds_read_b64_tr_b16 v[70:71], v37 offset:512\n\t"
  "s_waitcnt lgkmcnt(5)\n\t"
  "v_mfma_f32_32x32x16_bf16 v[96:111], v[88:91], v[156:159], v[96:111]\n\t"
  "ds_read_b128 v[164:167], v33\n\t"
  "ds_read_b64_tr_b16 v[72:73], v36 offset:2048\n\t"
  "ds_read_b64_tr_b16 v[74:75], v36 offset:2560\n\t"
  "v_mfma_f32_32x32x16_bf16 v[112:127], v[92:95], v[156:159], v[112:127]\n\t"
  "ds_read_b64_tr_b16 v[76:77], v37 offset:2048\n\t"
  "ds_read_b64_tr_b16 v[78:79], v37 offset:2560\n\t"
  "s_waitcnt lgkmcnt(5)\n\t"
  "v_mfma_f32_32x32x16_bf16 v[96:111], v[64:67], v[160:163], v[96:111]\n\t"
  "ds_read_b128 v[168:171], v34\n\t"
  "ds_read_b64_tr_b16 v[80:81], v36 offset:4096\n\t"
  "ds_read_b64_tr_b16 v[82:83], v36 offset:4608\n\t"
  "v_mfma_f32_32x32x16_bf16 v[112:127], v[68:71], v[160:163], v[112:127]\n\t"
  "ds_read_b64_tr_b16 v[84:85], v37 offset:4096\n\t"
  "ds_read_b64_tr_b16 v[86:87], v37 offset:4608\n\t"
  "v_add_u32 v32, 0x6000, v32\n\t"
  "v_add_u32 v34, 0x6000, v34\n\t"
  "s_waitcnt lgkmcnt(5)\n\t"
  "v_mfma_f32_32x32x16_bf16 v[96:111], v[72:75], v[164:167], v[96:111]\n\t"
  "ds_read_b128 v[172:175], v35\n\t"
  "ds_read_b64_tr_b16 v[88:89], v36 offset:6144\n\t"
  "ds_read_b64_tr_b16 v[90:91], v36 offset:6656\n\t"
  "v_mfma_f32_32x32x16_bf16 v[112:127], v[76:79], v[164:167], v[112:127]\n\t"
  "ds_read_b64_tr_b16 v[92:93], v37 offset:6144\n\t"
  "ds_read_b64_tr_b16 v[94:95], v37 offset:6656\n\t"
  "v_add_u32 v33, 0x6000, v33\n\t"
  "v_add_u32 v35, 0x6000, v35\n\t"
  "v_add_u32 v36, 0x6000, v36\n\t"
  "v_add_u32 v37, 0x6000, v37\n\t"
  "s_waitcnt lgkmcnt(5)\n\t"
  "v_mfma_f32_32x32x16_bf16 v[96:111], v[80:83], v[168:171], v[96:111]\n\t"
  "v_mfma_f32_32x32x16_bf16 v[112:127], v[84:87], v[168:171], v[112:127]\n\t"
  "s_barrier\n\t"
  "ds_read_b128 v[176:179], v32\n\t"
  "ds_read_b64_tr_b16 v[64:65], v36 offset:0\n\t"
  "ds_read_b64_tr_b16 v[66:67], v36 offset:512\n\t"
  "ds_read_b64_tr_b16 v[68:69], v37 offset:0\n\t"
  "ds_read_b64_tr_b16 v[70:71], v37 offset:512\n\t"
  "s_waitcnt lgkmcnt(5)\n\t"
  "v_mfma_f32_32x32x16_bf16 v[96:111], v[88:91], v[172:175], v[96:111]\n\t"
  "ds_read_b128 v[180:183], v33\n\t"
  "ds_read_b64_tr_b16 v[72:73], v36 offset:2048\n\t"
  "ds_read_b64_tr_b16 v[74:75], v36 offset:2560\n\t"
  "v_mfma_f32_32x32x16_bf16 v[112:127], v[92:95], v[172:175], v[112:127]\n\t"
  "ds_read_b64_tr_b16 v[76:77], v37 offset:2048\n\t"
  "ds_read_b64_tr_b16 v[78:79], v37 offset:2560\n\t"
  "s_waitcnt lgkmcnt(5)\n\t"
  "v_mfma_f32_32x32x16_bf16 v[96:111], v[64:67], v[176:179], v[96:111]\n\t"
  "ds_read_b128 v[184:187], v34\n\t"
  "ds_read_b64_tr_b16 v[80:81], v36 offset:4096\n\t"
  "ds_read_b64_tr_b16 v[82:83], v36 offset:4608\n\t"
  "v_mfma_f32_32x32x16_bf16 v[112:127], v[68:71], v[176:179], v[112:127]\n\t"
  "ds_read_b64_tr_b16 v[84:85], v37 offset:4096\n\t"
  "ds_read_b64_tr_b16 v[86:87], v37 offset:4608\n\t"
  "v_add_u32 v32, 0x6000, v32\n\t"
  "v_add_u32 v34, 0x6000, v34\n\t"
  "s_waitcnt lgkmcnt(5)\n\t"
  "v_mfma_f32_32x32x16_bf16 v[96:111], v[72:75], v[180:183], v[96:111]\n\t"
  "ds_read_b128 v[188:191], v35\n\t"
  "ds_read_b64_tr_b16 v[88:89], v36 offset:6144\n\t"
  "ds_read_b64_tr_b16 v[90:91], v36 offset:6656\n\t"
  "v_mfma_f32_32x32x16_bf16 v[112:127], v[76:79], v[180:183], v[112:127]\n\t"
  "ds_read_b64_tr_b16 v[92:93], v37 offset:6144\n\t"
  "ds_read_b64_tr_b16 v[94:95], v37 offset:6656\n\t"
  "v_add_u32 v33, 0x6000, v33\n\t"
  "v_add_u32 v35, 0x6000, v35\n\t"
  "v_add_u32 v36, 0x6000, v36\n\t"
  "v_add_u32 v37, 0x6000, v37\n\t"
  "s_waitcnt lgkmcnt(5)\n\t"
  "v_mfma_f32_32x32x16_bf16 v[96:111], v[80:83], v[184:187], v[96:111]\n\t"
  "v_mfma_f32_32x32x16_bf16 v[112:127], v[84:87], v[184:187], v[112:127]\n\t"
  "s_barrier\n\t"
  "ds_read_b128 v[192:195], v32\n\t"
  "ds_read_b64_tr_b16 v[64:65], v36 offset:0\n\t"
  "ds_read_b64_tr_b16 v[66:67], v36 offset:512\n\t"
  "ds_read_b64_tr_b16 v[68:69], v37 offset:0\n\t"
  "ds_read_b64_tr_b16 v[70:71], v37 offset:512\n\t"
  "s_waitcnt lgkmcnt(5)\n\t"
  "v_mfma_f32_32x32x16_bf16 v[96:111], v[88:91], v[188:191], v[96:111]\n\t"
  "ds_read_b128 v[196:199], v33\n\t"
  "ds_read_b64_tr_b16 v[72:73], v36 offset:2048\n\t"
  "ds_read_b64_tr_b16 v[74:75], v36 offset:2560\n\t"
  "v_mfma_f32_32x32x16_bf16 v[112:127], v[92:95], v[188:191], v[112:127]\n\t"
  "ds_read_b64_tr_b16 v[76:77], v37 offset:2048\n\t"
  "ds_read_b64_tr_b16 v[78:79], v37 offset:2560\n\t"
  "s_waitcnt lgkmcnt(5)\n\t"
  "v_mfma_f32_32x32x16_bf16 v[96:111], v[64:67], v[192:195], v[96:111]\n\t"
  "ds_read_b128 v[200:203], v34\n\t"
  "ds_read_b64_tr_b16 v[80:81], v36 offset:4096\n\t"
  "ds_read_b64_tr_b16 v[82:83], v36 offset:4608\n\t"
  "v_mfma_f32_32x32x16_bf16 v[112:127], v[68:71], v[192:195], v[112:127]\n\t"
  "ds_read_b64_tr_b16 v[84:85], v37 offset:4096\n\t"
  "ds_read_b64_tr_b16 v[86:87], v37 offset:4608\n\t"
  "v_add_u32 v32, 0x6000, v32\n\t"
  "v_add_u32 v34, 0x6000, v34\n\t"
  "s_waitcnt lgkmcnt(5)\n\t"
  "v_mfma_f32_32x32x16_bf16 v[96:111], v[72:75], v[196:199], v[96:111]\n\t"
  "ds_read_b128 v[204:207], v35\n\t"
  "ds_read_b64_tr_b16 v[88:89], v36 offset:6144\n\t"
  "ds_read_b64_tr_b16 v[90:91], v36 offset:6656\n\t"
  "v_mfma_f32_32x32x16_bf16 v[112:127], v[76:79], v[196:199], v[112:127]\n\t"
  "ds_read_b64_tr_b16 v[92:93], v37 offset:6144\n\t"
  "ds_read_b64_tr_b16 v[94:95], v37 offset:6656\n\t"
  "v_add_u32 v33, 0x6000, v33\n\t"
  "v_add_u32 v35, 0x6000, v35\n\t"
  "v_add_u32 v36, 0x6000, v36\n\t"
  "v_add_u32 v37, 0x6000, v37\n\t"
  "s_waitcnt lgkmcnt(5)\n\t"
  "v_mfma_f32_32x32x16_bf16 v[96:111], v[80:83], v[200:203], v[96:111]\n\t"
  "v_mfma_f32_32x32x16_bf16 v[112:127], v[84:87], v[200:203], v[112:127]\n\t"
  "s_barrier\n\t"
  "ds_read_b128 v[208:211], v32\n\t"
  "ds_read_b64_tr_b16 v[64:65], v36 offset:0\n\t"
  "ds_read_b64_tr_b16 v[66:67], v36 offset:512\n\t"
  "ds_read_b64_tr_b16 v[68:69], v37 offset:0\n\t"
  "ds_read_b64_tr_b16 v[70:71], v37 offset:512\n\t"
  "s_waitcnt lgkmcnt(5)\n\t"
  "v_mfma_f32_32x32x16_bf16 v[96:111], v[88:91], v[204:207], v[96:111]\n\t"
  "ds_read_b128 v[212:215], v33\n\t"
  "ds_read_b64_tr_b16 v[72:73], v36 offset:2048\n\t"
  "ds_read_b64_tr_b16 v[74:75], v36 offset:2560\n\t"
  "v_mfma_f32_32x32x16_bf16 v[112:127], v[92:95], v[204:207], v[112:127]\n\t"
  "ds_read_b64_tr_b16 v[76:77], v37 offset:2048\n\t"
  "ds_read_b64_tr_b16 v[78:79], v37 offset:2560\n\t"
  "s_waitcnt lgkmcnt(5)\n\t"
  "v_mfma_f32_32x32x16_bf16 v[96:111], v[64:67], v[208:211], v[96:111]\n\t"
  "ds_read_b128 v[216:219], v34\n\t"
  "ds_read_b64_tr_b16 v[80:81], v36 offset:4096\n\t"
  "ds_read_b64_tr_b16 v[82:83], v36 offset:4608\n\t"
  "v_mfma_f32_32x32x16_bf16 v[112:127], v[68:71], v[208:211], v[112:127]\n\t"
  "ds_read_b64_tr_b16 v[84:85], v37 offset:4096\n\t"
  "ds_read_b64_tr_b16 v[86:87], v37 offset:4608\n\t"
  "v_add_u32 v32, 0xfffe8000, v32\n\t"
  "v_add_u32 v34, 0xfffe8000, v34\n\t"
  "s_waitcnt lgkmcnt(5)\n\t"
  "v_mfma_f32_32x32x16_bf16 v[96:111], v[72:75], v[212:215], v[96:111]\n\t"
  "ds_read_b128 v[220:223], v35\n\t"
  "ds_read_b64_tr_b16 v[88:89], v36 offset:6144\n\t"
  "ds_read_b64_tr_b16 v[90:91], v36 offset:6656\n\t"
  "v_mfma_f32_32x32x16_bf16 v[112:127], v[76:79], v[212:215], v[112:127]\n\t"
  "ds_read_b64_tr_b16 v[92:93], v37 offset:6144\n\t"
  "ds_read_b64_tr_b16 v[94:95], v37 offset:6656\n\t"
  "v_add_u32 v33, 0xfffe8000, v33\n\t"
  "v_add_u32 v35, 0xfffe8000, v35\n\t"
  "v_add_u32 v36, 0xfffe8000, v36\n\t"
  "v_add_u32 v37, 0xfffe8000, v37\n\t"
  "s_waitcnt lgkmcnt(5)\n\t"
  "v_mfma_f32_32x32x16_bf16 v[96:111], v[80:83], v[216:219], v[96:111]\n\t"
  "v_mfma_f32_32x32x16_bf16 v[112:127], v[84:87], v[216:219], v[112:127]\n\t"
  "s_barrier\n\t"
  "ds_read_b128 v[224:227], v32\n\t"
  "ds_read_b64_tr_b16 v[64:65], v36 offset:0\n\t"
  "ds_read_b64_tr_b16 v[66:67], v36 offset:512\n\t"
  "ds_read_b64_tr_b16 v[68:69], v37 offset:0\n\t"
  "ds_read_b64_tr_b16 v[70:71], v37 offset:512\n\t"
  "s_waitcnt lgkmcnt(5)\n\t"
  "v_mfma_f32_32x32x16_bf16 v[96:111], v[88:91], v[220:223], v[96:111]\n\t"
  "ds_read_b128 v[228:231], v33\n\t"
  "ds_read_b64_tr_b16 v[72:73], v36 offset:2048\n\t"
  "ds_read_b64_tr_b16 v[74:75], v36 offset:2560\n\t"
  "v_mfma_f32_32x32x16_bf16 v[112:127], v[92:95], v[220:223], v[112:127]\n\t"
  "ds_read_b64_tr_b16 v[76:77], v37 offset:2048\n\t"
  "ds_read_b64_tr_b16 v[78:79], v37 offset:2560\n\t"
  "s_waitcnt lgkmcnt(5)\n\t"
  "v_mfma_f32_32x32x16_bf16 v[96:111], v[64:67], v[224:227], v[96:111]\n\t"
  "ds_read_b128 v[232:235], v34\n\t"
  "ds_read_b64_tr_b16 v[80:81], v36 offset:4096\n\t"
  "ds_read_b64_tr_b16 v[82:83], v36 offset:4608\n\t"
  "v_mfma_f32_32x32x16_bf16 v[112:127], v[68:71], v[224:227], v[112:127]\n\t"
  "ds_read_b64_tr_b16 v[84:85], v37 offset:4096\n\t"
  "ds_read_b64_tr_b16 v[86:87], v37 offset:4608\n\t"
  "v_add_u32 v32, 0x6000, v32\n\t"
  "v_add_u32 v34, 0x6000, v34\n\t"
  "s_waitcnt lgkmcnt(5)\n\t"
  "v_mfma_f32_32x32x16_bf16 v[96:111], v[72:75], v[228:231], v[96:111]\n\t"
  "ds_read_b128 v[236:239], v35\n\t"
  "ds_read_b64_tr_b16 v[88:89], v36 offset:6144\n\t"
  "ds_read_b64_tr_b16 v[90:91], v36 offset:6656\n\t"
  "v_mfma_f32_32x32x16_bf16 v[112:127], v[76:79], v[228:231], v[112:127]\n\t"
  "ds_read_b64_tr_b16 v[92:93], v37 offset:6144\n\t"
  "ds_read_b64_tr_b16 v[94:95], v37 offset:6656\n\t"
  "v_add_u32 v33, 0x6000, v33\n\t"
  "v_add_u32 v35, 0x6000, v35\n\t"
  "v_add_u32 v36, 0x6000, v36\n\t"
  "v_add_u32 v37, 0x6000, v37\n\t"
  "s_waitcnt lgkmcnt(5)\n\t"
  "v_mfma_f32_32x32x16_bf16 v[96:111], v[80:83], v[232:235], v[96:111]\n\t"
  "v_mfma_f32_32x32x16_bf16 v[112:127], v[84:87], v[232:235], v[112:127]\n\t"
  "s_barrier\n\t"
  "ds_read_b128 v[240:243], v32\n\t"
  "ds_read_b64_tr_b16 v[64:65], v36 offset:0\n\t"
  "ds_read_b64_tr_b16 v[66:67], v36 offset:512\n\t"
  "ds_read_b64_tr_b16 v[68:69], v37 offset:0\n\t"
  "ds_read_b64_tr_b16 v[70:71], v37 offset:512\n\t"
  "s_waitcnt lgkmcnt(5)\n\t"
  "v_mfma_f32_32x32x16_bf16 v[96:111], v[88:91], v[236:239], v[96:111]\n\t"
  "ds_read_b128 v[244:247], v33\n\t"
  "ds_read_b64_tr_b16 v[72:73], v36 offset:2048\n\t"
  "ds_read_b64_tr_b16 v[74:75], v36 offset:2560\n\t"
  "v_mfma_f32_32x32x16_bf16 v[112:127], v[92:95], v[236:239], v[112:127]\n\t"
  "ds_read_b64_tr_b16 v[76:77], v37 offset:2048\n\t"
  "ds_read_b64_tr_b16 v[78:79], v37 offset:2560\n\t"
  "s_waitcnt lgkmcnt(5)\n\t"
  "v_mfma_f32_32x32x16_bf16 v[96:111], v[64:67], v[240:243], v[96:111]\n\t"
  "ds_read_b128 v[248:251], v34\n\t"
  "ds_read_b64_tr_b16 v[80:81], v36 offset:4096\n\t"
  "ds_read_b64_tr_b16 v[82:83], v36 offset:4608\n\t"
  "v_mfma_f32_32x32x16_bf16 v[112:127], v[68:71], v[240:243], v[112:127]\n\t"
  "ds_read_b64_tr_b16 v[84:85], v37 offset:4096\n\t"
  "ds_read_b64_tr_b16 v[86:87], v37 offset:4608\n\t"
  "s_waitcnt lgkmcnt(5)\n\t"
  "v_mfma_f32_32x32x16_bf16 v[96:111], v[72:75], v[244:247], v[96:111]\n\t"
  "ds_read_b128 v[252:255], v35\n\t"
  "ds_read_b64_tr_b16 v[88:89], v36 offset:6144\n\t"
  "ds_read_b64_tr_b16 v[90:91], v36 offset:6656\n\t"
  "v_mfma_f32_32x32x16_bf16 v[112:127], v[76:79], v[244:247], v[112:127]\n\t"
  "ds_read_b64_tr_b16 v[92:93], v37 offset:6144\n\t"
  "ds_read_b64_tr_b16 v[94:95], v37 offset:6656\n\t"
  "s_waitcnt lgkmcnt(5)\n\t"
  "v_mfma_f32_32x32x16_bf16 v[96:111], v[80:83], v[248:251], v[96:111]\n\t"
  "v_mfma_f32_32x32x16_bf16 v[112:127], v[84:87], v[248:251], v[112:127]\n\t"
  "s_waitcnt lgkmcnt(0)\n\t"
  "v_mfma_f32_32x32x16_bf16 v[96:111], v[88:91], v[252:255], v[96:111]\n\t"
  "v_mfma_f32_32x32x16_bf16 v[112:127], v[92:95], v[252:255], v[112:127]\n\t"
  "s_nop 10\n\t"
  "v_cvt_pk_bf16_f32 v64, v96, v97\n\t"
  "v_cvt_pk_bf16_f32 v65, v98, v99\n\t"
  "v_cvt_pk_bf16_f32 v66, v100, v101\n\t"
  "v_cvt_pk_bf16_f32 v67, v102, v103\n\t"
  "buffer_store_dwordx4 v[64:67], v39, s[36:39], s45 offen offset:0 sc1\n\t"
  "v_cvt_pk_bf16_f32 v68, v104, v105\n\t"
  "v_cvt_pk_bf16_f32 v69, v106, v107\n\t"
  "v_cvt_pk_bf16_f32 v70, v108, v109\n\t"
  "v_cvt_pk_bf16_f32 v71, v110, v111\n\t"
  "buffer_store_dwordx4 v[68:71], v39, s[36:39], s45 offen offset:1024 sc1\n\t"
  "v_cvt_pk_bf16_f32 v72, v112, v113\n\t"
  "v_cvt_pk_bf16_f32 v73, v114, v115\n\t"
  "v_cvt_pk_bf16_f32 v74, v116, v117\n\t"
  "v_cvt_pk_bf16_f32 v75, v118, v119\n\t"
  "buffer_store_dwordx4 v[72:75], v39, s[36:39], s46 offen offset:0 sc1\n\t"
  "v_cvt_pk_bf16_f32 v76, v120, v121\n\t"
  "v_cvt_pk_bf16_f32 v77, v122, v123\n\t"
  "v_cvt_pk_bf16_f32 v78, v124, v125\n\t"
  "v_cvt_pk_bf16_f32 v79, v126, v127\n\t"
  "buffer_store_dwordx4 v[76:79], v39, s[36:39], s46 offen offset:1024 sc1\n\t"
  "s_barrier\n\t"
  "ds_read_b64 v[56:57], v54 offset:96\n\t"
  "v_mul_f32 v12, v96, v96\n\t"
  "v_mul_f32 v13, v112, v112\n\t"
  "v_mul_f32 v14, v108, v108\n\t"
  "v_mul_f32 v15, v124, v124\n\t"
  "v_fmac_f32 v12, v97, v97\n\t"
  "v_fmac_f32 v13, v113, v113\n\t"
  "v_fmac_f32 v14, v109, v109\n\t"
  "v_fmac_f32 v15, v125, v125\n\t"
  "v_fmac_f32 v12, v98, v98\n\t"
  "v_fmac_f32 v13, v114, v114\n\t"
  "v_fmac_f32 v14, v110, v110\n\t"
  "v_fmac_f32 v15, v126, v126\n\t"
  "v_fmac_f32 v12, v99, v99\n\t"
  "v_fmac_f32 v13, v115, v115\n\t"
  "v_fmac_f32 v14, v111, v111\n\t"
  "v_fmac_f32 v15, v127, v127\n\t"
  "v_fmac_f32 v12, v100, v100\n\t"
  "v_fmac_f32 v13, v116, v116\n\t"
  "v_fmac_f32 v12, v101, v101\n\t"
  "v_fmac_f32 v13, v117, v117\n\t"
  "v_fmac_f32 v12, v102, v102\n\t"
  "v_fmac_f32 v13, v118, v118\n\t"
  "v_fmac_f32 v12, v103, v103\n\t"
  "v_fmac_f32 v13, v119, v119\n\t"
  "v_fmac_f32 v12, v104, v104\n\t"
  "v_fmac_f32 v13, v120, v120\n\t"
  "v_fmac_f32 v12, v105, v105\n\t"
  "v_fmac_f32 v13, v121, v121\n\t"
  "v_fmac_f32 v12, v106, v106\n\t"
  "v_fmac_f32 v13, v122, v122\n\t"
  "v_fmac_f32 v12, v107, v107\n\t"
  "v_fmac_f32 v13, v123, v123\n\t"
  "v_cndmask_b32 v14, 0, v14, s[60:61]\n\t"
  "v_cndmask_b32 v15, 0, v15, s[60:61]\n\t"
  "v_add_f32 v12, v12, v14\n\t"
  "v_add_f32 v13, v13, v15\n\t"
  "v_mov_b32 v16, v12\n\t"
  "v_mov_b32 v17, v13\n\t"
  "s_nop 1\n\t"
  "s_nop 1\n\t"
  "v_permlane32_swap_b32 v12, v16\n\t"
  "v_permlane32_swap_b32 v13, v17\n\t"
  "s_nop 0\n\t"
  "s_nop 0\n\t"
  "v_add_f32 v12, v12, v16\n\t"
  "v_add_f32 v13, v13, v17\n\t"
  "s_nop 1\n\t"
  "v_max_u32_dpp v12, v12, v12 row_shr:1\n\t"
  "v_max_u32_dpp v13, v13, v13 row_shr:1\n\t"
  "s_nop 0\n\t"
  "v_max_u32_dpp v12, v12, v12 row_shr:2\n\t"
  "v_max_u32_dpp v13, v13, v13 row_shr:2\n\t"
  "s_nop 0\n\t"
  "v_max_u32_dpp v12, v12, v12 row_shr:4\n\t"
  "v_max_u32_dpp v13, v13, v13 row_shr:4\n\t"
  "s_nop 0\n\t"
  "v_max_u32_dpp v12, v12, v12 row_shr:8\n\t"
  "v_max_u32_dpp v13, v13, v13 row_shr:8\n\t"
  "s_nop 0\n\t"
  "v_max_u32_dpp v12, v12, v12 row_bcast:15 row_mask:0xa\n\t"
  "v_max_u32_dpp v13, v13, v13 row_bcast:15 row_mask:0xa\n\t"
  "s_nop 0\n\t"
  "v_max_u32_dpp v12, v12, v12 row_bcast:31 row_mask:0xc\n\t"
  "v_max_u32_dpp v13, v13, v13 row_bcast:31 row_mask:0xc\n\t"
  "s_nop 0\n\t"
  "s_mov_b32 exec_lo, 0\n\t"
  "s_brev_b32 exec_hi, 1\n\t"
  "ds_max_u32 v54, v12 offset:64\n\t"
  "ds_max_u32 v54, v13 offset:68\n\t"
  "s_mov_b64 exec, -1\n\t"
  "s_branch LJ_%=\n\t"
  "LV_%=:\n\t"
  "s_barrier\n\t"
  "ds_read_b128 v[144:147], v32\n\t"
  "ds_read_b64_tr_b16 v[64:65], v36 offset:0\n\t"
  "ds_read_b64_tr_b16 v[66:67], v36 offset:512\n\t"
  "ds_read_b64_tr_b16 v[68:69], v37 offset:0\n\t"
  "ds_read_b64_tr_b16 v[70:71], v37 offset:512\n\t"
  "ds_read_b128 v[148:151], v33\n\t"
  "ds_read_b64_tr_b16 v[72:73], v36 offset:2048\n\t"
  "ds_read_b64_tr_b16 v[74:75], v36 offset:2560\n\t"
  "ds_read_b64_tr_b16 v[76:77], v37 offset:2048\n\t"
  "ds_read_b64_tr_b16 v[78:79], v37 offset:2560\n\t"
  "s_waitcnt lgkmcnt(5)\n\t"
  "v_mfma_f32_32x32x16_bf16 v[96:111], v[144:147], v[64:67], 0\n\t"
  "ds_read_b128 v[152:155], v34\n\t"
  "ds_read_b64_tr_b16 v[80:81], v36 offset:4096\n\t"
  "ds_read_b64_tr_b16 v[82:83], v36 offset:4608\n\t"
  "v_mfma_f32_32x32x16_bf16 v[112:127], v[144:147], v[68:71], 0\n\t"
  "ds_read_b64_tr_b16 v[84:85], v37 offset:4096\n\t"
  "ds_read_b64_tr_b16 v[86:87], v37 offset:4608\n\t"
  "v_add_u32 v32, 0x6000, v32\n\t"
  "v_add_u32 v34, 0x6000, v34\n\t"
  "s_waitcnt lgkmcnt(5)\n\t"
  "v_mfma_f32_32x32x16_bf16 v[96:111], v[148:151], v[72:75], v[96:111]\n\t"
  "ds_read_b128 v[156:159], v35\n\t"
  "ds_read_b64_tr_b16 v[88:89], v36 offset:6144\n\t"
  "ds_read_b64_tr_b16 v[90:91], v36 offset:6656\n\t"
  "v_mfma_f32_32x32x16_bf16 v[112:127], v[148:151], v[76:79], v[112:127]\n\t"
  "ds_read_b64_tr_b16 v[92:93], v37 offset:6144\n\t"
  "ds_read_b64_tr_b16 v[94:95], v37 offset:6656\n\t"
  "v_add_u32 v33, 0x6000, v33\n\t"
  "v_add_u32 v35, 0x6000, v35\n\t"
  "v_add_u32 v36, 0x6000, v36\n\t"
  "v_add_u32 v37, 0x6000, v37\n\t"
  "s_waitcnt lgkmcnt(5)\n\t"
  "v_mfma_f32_32x32x16_bf16 v[96:111], v[152:155], v[80:83], v[96:111]\n\t"
  "v_mfma_f32_32x32x16_bf16 v[112:127], v[152:155], v[84:87], v[112:127]\n\t"
  "s_barrier\n\t"
  "ds_read_b128 v[160:163], v32\n\t"
  "ds_read_b64_tr_b16 v[64:65], v36 offset:0\n\t"
  "ds_read_b64_tr_b16 v[66:67], v36 offset:512\n\t"
  "ds_read_b64_tr_b16 v[68:69], v37 offset:0\n\t"
  "ds_read_b64_tr_b16 v[70:71], v37 offset:512\n\t"
  "s_waitcnt lgkmcnt(5)\n\t"
  "v_mfma_f32_32x32x16_bf16 v[96:111], v[156:159], v[88:91], v[96:111]\n\t"
  "ds_read_b128 v[164:167], v33\n\t"
  "ds_read_b64_tr_b16 v[72:73], v36 offset:2048\n\t"
  "ds_read_b64_tr_b16 v[74:75], v36 offset:2560\n\t"
  "v_mfma_f32_32x32x16_bf16 v[112:127], v[156:159], v[92:95], v[112:127]\n\t"
  "ds_read_b64_tr_b16 v[76:77], v37 offset:2048\n\t"
  "ds_read_b64_tr_b16 v[78:79], v37 offset:2560\n\t"
  "s_waitcnt lgkmcnt(5)\n\t"
  "v_mfma_f32_32x32x16_bf16 v[96:111], v[160:163], v[64:67], v[96:111]\n\t"
  "ds_read_b128 v[168:171], v34\n\t"
  "ds_read_b64_tr_b16 v[80:81], v36 offset:4096\n\t"
  "ds_read_b64_tr_b16 v[82:83], v36 offset:4608\n\t"
  "v_mfma_f32_32x32x16_bf16 v[112:127], v[160:163], v[68:71], v[112:127]\n\t"
  "ds_read_b64_tr_b16 v[84:85], v37 offset:4096\n\t"
  "ds_read_b64_tr_b16 v[86:87], v37 offset:4608\n\t"
  "v_add_u32 v32, 0x6000, v32\n\t"
  "v_add_u32 v34, 0x6000, v34\n\t"
  "s_waitcnt lgkmcnt(5)\n\t"
  "v_mfma_f32_32x32x16_bf16 v[96:111], v[164:167], v[72:75], v[96:111]\n\t"
  "ds_read_b128 v[172:175], v35\n\t"
  "ds_read_b64_tr_b16 v[88:89], v36 offset:6144\n\t"
  "ds_read_b64_tr_b16 v[90:91], v36 offset:6656\n\t"
  "v_mfma_f32_32x32x16_bf16 v[112:127], v[164:167], v[76:79], v[112:127]\n\t"
  "ds_read_b64_tr_b16 v[92:93], v37 offset:6144\n\t"
  "ds_read_b64_tr_b16 v[94:95], v37 offset:6656\n\t"
  "v_add_u32 v33, 0x6000, v33\n\t"
  "v_add_u32 v35, 0x6000, v35\n\t"
  "v_add_u32 v36, 0x6000, v36\n\t"
  "v_add_u32 v37, 0x6000, v37\n\t"
  "s_waitcnt lgkmcnt(5)\n\t"
  "v_mfma_f32_32x32x16_bf16 v[96:111], v[168:171], v[80:83], v[96:111]\n\t"
  "v_mfma_f32_32x32x16_bf16 v[112:127], v[168:171], v[84:87], v[112:127]\n\t"
  "s_barrier\n\t"
  "ds_read_b128 v[176:179], v32\n\t"
  "ds_read_b64_tr_b16 v[64:65], v36 offset:0\n\t"
  "ds_read_b64_tr_b16 v[66:67], v36 offset:512\n\t"
  "ds_read_b64_tr_b16 v[68:69], v37 offset:0\n\t"
  "ds_read_b64_tr_b16 v[70:71], v37 offset:512\n\t"
  "s_waitcnt lgkmcnt(5)\n\t"
  "v_mfma_f32_32x32x16_bf16 v[96:111], v[172:175], v[88:91], v[96:111]\n\t"
  "ds_read_b128 v[180:183], v33\n\t"
  "ds_read_b64_tr_b16 v[72:73], v36 offset:2048\n\t"
  "ds_read_b64_tr_b16 v[74:75], v36 offset:2560\n\t"
  "v_mfma_f32_32x32x16_bf16 v[112:127], v[172:175], v[92:95], v[112:127]\n\t"
  "ds_read_b64_tr_b16 v[76:77], v37 offset:2048\n\t"
  "ds_read_b64_tr_b16 v[78:79], v37 offset:2560\n\t"
  "s_waitcnt lgkmcnt(5)\n\t"
  "v_mfma_f32_32x32x16_bf16 v[96:111], v[176:179], v[64:67], v[96:111]\n\t"
  "ds_read_b128 v[184:187], v34\n\t"
  "ds_read_b64_tr_b16 v[80:81], v36 offset:4096\n\t"
  "ds_read_b64_tr_b16 v[82:83], v36 offset:4608\n\t"
  "v_mfma_f32_32x32x16_bf16 v[112:127], v[176:179], v[68:71], v[112:127]\n\t"
  "ds_read_b64_tr_b16 v[84:85], v37 offset:4096\n\t"
  "ds_read_b64_tr_b16 v[86:87], v37 offset:4608\n\t"
  "v_add_u32 v32, 0x6000, v32\n\t"
  "v_add_u32 v34, 0x6000, v34\n\t"
  "s_waitcnt lgkmcnt(5)\n\t"
  "v_mfma_f32_32x32x16_bf16 v[96:111], v[180:183], v[72:75], v[96:111]\n\t"
  "ds_read_b128 v[188:191], v35\n\t"
  "ds_read_b64_tr_b16 v[88:89], v36 offset:6144\n\t"
  "ds_read_b64_tr_b16 v[90:91], v36 offset:6656\n\t"
  "v_mfma_f32_32x32x16_bf16 v[112:127], v[180:183], v[76:79], v[112:127]\n\t"
  "ds_read_b64_tr_b16 v[92:93], v37 offset:6144\n\t"
  "ds_read_b64_tr_b16 v[94:95], v37 offset:6656\n\t"
  "v_add_u32 v33, 0x6000, v33\n\t"
  "v_add_u32 v35, 0x6000, v35\n\t"
  "v_add_u32 v36, 0x6000, v36\n\t"
  "v_add_u32 v37, 0x6000, v37\n\t"
  "s_waitcnt lgkmcnt(5)\n\t"
  "v_mfma_f32_32x32x16_bf16 v[96:111], v[184:187], v[80:83], v[96:111]\n\t"
  "v_mfma_f32_32x32x16_bf16 v[112:127], v[184:187], v[84:87], v[112:127]\n\t"
  "s_barrier\n\t"
  "ds_read_b128 v[192:195], v32\n\t"
  "ds_read_b64_tr_b16 v[64:65], v36 offset:0\n\t"
  "ds_read_b64_tr_b16 v[66:67], v36 offset:512\n\t"
  "ds_read_b64_tr_b16 v[68:69], v37 offset:0\n\t"
  "ds_read_b64_tr_b16 v[70:71], v37 offset:512\n\t"
  "s_waitcnt lgkmcnt(5)\n\t"
  "v_mfma_f32_32x32x16_bf16 v[96:111], v[188:191], v[88:91], v[96:111]\n\t"
  "ds_read_b128 v[196:199], v33\n\t"
  "ds_read_b64_tr_b16 v[72:73], v36 offset:2048\n\t"
  "ds_read_b64_tr_b16 v[74:75], v36 offset:2560\n\t"
  "v_mfma_f32_32x32x16_bf16 v[112:127], v[188:191], v[92:95], v[112:127]\n\t"
  "ds_read_b64_tr_b16 v[76:77], v37 offset:2048\n\t"
  "ds_read_b64_tr_b16 v[78:79], v37 offset:2560\n\t"
  "s_waitcnt lgkmcnt(5)\n\t"
  "v_mfma_f32_32x32x16_bf16 v[96:111], v[192:195], v[64:67], v[96:111]\n\t"
  "ds_read_b128 v[200:203], v34\n\t"
  "ds_read_b64_tr_b16 v[80:81], v36 offset:4096\n\t"
  "ds_read_b64_tr_b16 v[82:83], v36 offset:4608\n\t"
  "v_mfma_f32_32x32x16_bf16 v[112:127], v[192:195], v[68:71], v[112:127]\n\t"
  "ds_read_b64_tr_b16 v[84:85], v37 offset:4096\n\t"
  "ds_read_b64_tr_b16 v[86:87], v37 offset:4608\n\t"
  "v_add_u32 v32, 0x6000, v32\n\t"
  "v_add_u32 v34, 0x6000, v34\n\t"
  "s_waitcnt lgkmcnt(5)\n\t"
  "v_mfma_f32_32x32x16_bf16 v[96:111], v[196:199], v[72:75], v[96:111]\n\t"
  "ds_read_b128 v[204:207], v35\n\t"
  "ds_read_b64_tr_b16 v[88:89], v36 offset:6144\n\t"
  "ds_read_b64_tr_b16 v[90:91], v36 offset:6656\n\t"
  "v_mfma_f32_32x32x16_bf16 v[112:127], v[196:199], v[76:79], v[112:127]\n\t"
  "ds_read_b64_tr_b16 v[92:93], v37 offset:6144\n\t"
  "ds_read_b64_tr_b16 v[94:95], v37 offset:6656\n\t"
  "v_add_u32 v33, 0x6000, v33\n\t"
  "v_add_u32 v35, 0x6000, v35\n\t"
  "v_add_u32 v36, 0x6000, v36\n\t"
  "v_add_u32 v37, 0x6000, v37\n\t"
  "s_waitcnt lgkmcnt(5)\n\t"
  "v_mfma_f32_32x32x16_bf16 v[96:111], v[200:203], v[80:83], v[96:111]\n\t"
  "v_mfma_f32_32x32x16_bf16 v[112:127], v[200:203], v[84:87], v[112:127]\n\t"
  "s_barrier\n\t"
  "ds_read_b128 v[208:211], v32\n\t"
  "ds_read_b64_tr_b16 v[64:65], v36 offset:0\n\t"
  "ds_read_b64_tr_b16 v[66:67], v36 offset:512\n\t"
  "ds_read_b64_tr_b16 v[68:69], v37 offset:0\n\t"
  "ds_read_b64_tr_b16 v[70:71], v37 offset:512\n\t"
  "s_waitcnt lgkmcnt(5)\n\t"
  "v_mfma_f32_32x32x16_bf16 v[96:111], v[204:207], v[88:91], v[96:111]\n\t"
  "ds_read_b128 v[212:215], v33\n\t"
  "ds_read_b64_tr_b16 v[72:73], v36 offset:2048\n\t"
  "ds_read_b64_tr_b16 v[74:75], v36 offset:2560\n\t"
  "v_mfma_f32_32x32x16_bf16 v[112:127], v[204:207], v[92:95], v[112:127]\n\t"
  "ds_read_b64_tr_b16 v[76:77], v37 offset:2048\n\t"
  "ds_read_b64_tr_b16 v[78:79], v37 offset:2560\n\t"
  "s_waitcnt lgkmcnt(5)\n\t"
  "v_mfma_f32_32x32x16_bf16 v[96:111], v[208:211], v[64:67], v[96:111]\n\t"
  "ds_read_b128 v[216:219], v34\n\t"
  "ds_read_b64_tr_b16 v[80:81], v36 offset:4096\n\t"
  "ds_read_b64_tr_b16 v[82:83], v36 offset:4608\n\t"
  "v_mfma_f32_32x32x16_bf16 v[112:127], v[208:211], v[68:71], v[112:127]\n\t"
  "ds_read_b64_tr_b16 v[84:85], v37 offset:4096\n\t"
  "ds_read_b64_tr_b16 v[86:87], v37 offset:4608\n\t"
  "v_add_u32 v32, 0xfffe8000, v32\n\t"
  "v_add_u32 v34, 0xfffe8000, v34\n\t"
  "s_waitcnt lgkmcnt(5)\n\t"
  "v_mfma_f32_32x32x16_bf16 v[96:111], v[212:215], v[72:75], v[96:111]\n\t"
  "ds_read_b128 v[220:223], v35\n\t"
  "ds_read_b64_tr_b16 v[88:89], v36 offset:6144\n\t"
  "ds_read_b64_tr_b16 v[90:91], v36 offset:6656\n\t"
  "v_mfma_f32_32x32x16_bf16 v[112:127], v[212:215], v[76:79], v[112:127]\n\t"
  "ds_read_b64_tr_b16 v[92:93], v37 offset:6144\n\t"
  "ds_read_b64_tr_b16 v[94:95], v37 offset:6656\n\t"
  "v_add_u32 v33, 0xfffe8000, v33\n\t"
  "v_add_u32 v35, 0xfffe8000, v35\n\t"
  "v_add_u32 v36, 0xfffe8000, v36\n\t"
  "v_add_u32 v37, 0xfffe8000, v37\n\t"
  "s_waitcnt lgkmcnt(5)\n\t"
  "v_mfma_f32_32x32x16_bf16 v[96:111], v[216:219], v[80:83], v[96:111]\n\t"
  "v_mfma_f32_32x32x16_bf16 v[112:127], v[216:219], v[84:87], v[112:127]\n\t"
  "s_barrier\n\t"
  "ds_read_b128 v[224:227], v32\n\t"
  "ds_read_b64_tr_b16 v[64:65], v36 offset:0\n\t"
  "ds_read_b64_tr_b16 v[66:67], v36 offset:512\n\t"
  "ds_read_b64_tr_b16 v[68:69], v37 offset:0\n\t"
  "ds_read_b64_tr_b16 v[70:71], v37 offset:512\n\t"
  "s_waitcnt lgkmcnt(5)\n\t"
  "v_mfma_f32_32x32x16_bf16 v[96:111], v[220:223], v[88:91], v[96:111]\n\t"
  "ds_read_b128 v[228:231], v33\n\t"
  "ds_read_b64_tr_b16 v[72:73], v36 offset:2048\n\t"
  "ds_read_b64_tr_b16 v[74:75], v36 offset:2560\n\t"
  "v_mfma_f32_32x32x16_bf16 v[112:127], v[220:223], v[92:95], v[112:127]\n\t"
  "ds_read_b64_tr_b16 v[76:77], v37 offset:2048\n\t"
  "ds_read_b64_tr_b16 v[78:79], v37 offset:2560\n\t"
  "s_waitcnt lgkmcnt(5)\n\t"
  "v_mfma_f32_32x32x16_bf16 v[96:111], v[224:227], v[64:67], v[96:111]\n\t"
  "ds_read_b128 v[232:235], v34\n\t"
  "ds_read_b64_tr_b16 v[80:81], v36 offset:4096\n\t"
  "ds_read_b64_tr_b16 v[82:83], v36 offset:4608\n\t"
  "v_mfma_f32_32x32x16_bf16 v[112:127], v[224:227], v[68:71], v[112:127]\n\t"
  "ds_read_b64_tr_b16 v[84:85], v37 offset:4096\n\t"
  "ds_read_b64_tr_b16 v[86:87], v37 offset:4608\n\t"
  "v_add_u32 v32, 0x6000, v32\n\t"
  "v_add_u32 v34, 0x6000, v34\n\t"
  "s_waitcnt lgkmcnt(5)\n\t"
  "v_mfma_f32_32x32x16_bf16 v[96:111], v[228:231], v[72:75], v[96:111]\n\t"
  "ds_read_b128 v[236:239], v35\n\t"
  "ds_read_b64_tr_b16 v[88:89], v36 offset:6144\n\t"
  "ds_read_b64_tr_b16 v[90:91], v36 offset:6656\n\t"
  "v_mfma_f32_32x32x16_bf16 v[112:127], v[228:231], v[76:79], v[112:127]\n\t"
  "ds_read_b64_tr_b16 v[92:93], v37 offset:6144\n\t"
  "ds_read_b64_tr_b16 v[94:95], v37 offset:6656\n\t"
  "v_add_u32 v33, 0x6000, v33\n\t"
  "v_add_u32 v35, 0x6000, v35\n\t"
  "v_add_u32 v36, 0x6000, v36\n\t"
  "v_add_u32 v37, 0x6000, v37\n\t"
  "s_waitcnt lgkmcnt(5)\n\t"
  "v_mfma_f32_32x32x16_bf16 v[96:111], v[232:235], v[80:83], v[96:111]\n\t"
  "v_mfma_f32_32x32x16_bf16 v[112:127], v[232:235], v[84:87], v[112:127]\n\t"
  "s_barrier\n\t"
  "ds_read_b128 v[240:243], v32\n\t"
  "ds_read_b64_tr_b16 v[64:65], v36 offset:0\n\t"
  "ds_read_b64_tr_b16 v[66:67], v36 offset:512\n\t"
  "ds_read_b64_tr_b16 v[68:69], v37 offset:0\n\t"
  "ds_read_b64_tr_b16 v[70:71], v37 offset:512\n\t"
  "s_waitcnt lgkmcnt(5)\n\t"
  "v_mfma_f32_32x32x16_bf16 v[96:111], v[236:239], v[88:91], v[96:111]\n\t"
  "ds_read_b128 v[244:247], v33\n\t"
  "ds_read_b64_tr_b16 v[72:73], v36 offset:2048\n\t"
  "ds_read_b64_tr_b16 v[74:75], v36 offset:2560\n\t"
  "v_mfma_f32_32x32x16_bf16 v[112:127], v[236:239], v[92:95], v[112:127]\n\t"
  "ds_read_b64_tr_b16 v[76:77], v37 offset:2048\n\t"
  "ds_read_b64_tr_b16 v[78:79], v37 offset:2560\n\t"
  "s_waitcnt lgkmcnt(5)\n\t"
  "v_mfma_f32_32x32x16_bf16 v[96:111], v[240:243], v[64:67], v[96:111]\n\t"
  "ds_read_b128 v[248:251], v34\n\t"
  "ds_read_b64_tr_b16 v[80:81], v36 offset:4096\n\t"
  "ds_read_b64_tr_b16 v[82:83], v36 offset:4608\n\t"
  "v_mfma_f32_32x32x16_bf16 v[112:127], v[240:243], v[68:71], v[112:127]\n\t"
  "ds_read_b64_tr_b16 v[84:85], v37 offset:4096\n\t"
  "ds_read_b64_tr_b16 v[86:87], v37 offset:4608\n\t"
  "s_waitcnt lgkmcnt(5)\n\t"
  "v_mfma_f32_32x32x16_bf16 v[96:111], v[244:247], v[72:75], v[96:111]\n\t"
  "ds_read_b128 v[252:255], v35\n\t"
  "ds_read_b64_tr_b16 v[88:89], v36 offset:6144\n\t"
  "ds_read_b64_tr_b16 v[90:91], v36 offset:6656\n\t"
  "v_mfma_f32_32x32x16_bf16 v[112:127], v[244:247], v[76:79], v[112:127]\n\t"
  "ds_read_b64_tr_b16 v[92:93], v37 offset:6144\n\t"
  "ds_read_b64_tr_b16 v[94:95], v37 offset:6656\n\t"
  "s_waitcnt lgkmcnt(5)\n\t"
  "v_mfma_f32_32x32x16_bf16 v[96:111], v[248:251], v[80:83], v[96:111]\n\t"
  "v_mfma_f32_32x32x16_bf16 v[112:127], v[248:251], v[84:87], v[112:127]\n\t"
  "s_waitcnt lgkmcnt(0)\n\t"
  "v_mfma_f32_32x32x16_bf16 v[96:111], v[252:255], v[88:91], v[96:111]\n\t"
  "v_mfma_f32_32x32x16_bf16 v[112:127], v[252:255], v[92:95], v[112:127]\n\t"
  "s_nop 10\n\t"
  "v_cvt_pk_bf16_f32 v64, v96, v97\n\t"
  "v_cvt_pk_bf16_f32 v65, v98, v99\n\t"
  "v_cvt_pk_bf16_f32 v66, v100, v101\n\t"
  "v_cvt_pk_bf16_f32 v67, v102, v103\n\t"
  "v_cndmask_b32 v64, v53, v64, s[58:59]\n\t"
  "v_cndmask_b32 v65, v53, v65, s[58:59]\n\t"
  "v_cndmask_b32 v66, v53, v66, s[58:59]\n\t"
  "v_cndmask_b32 v67, v53, v67, s[58:59]\n\t"
  "buffer_store_dwordx4 v[64:67], v39, s[36:39], s45 offen offset:0 sc1\n\t"
  "v_cvt_pk_bf16_f32 v68, v104, v105\n\t"
  "v_cvt_pk_bf16_f32 v69, v106, v107\n\t"
  "v_cvt_pk_bf16_f32 v70, v108, v109\n\t"
  "v_cvt_pk_bf16_f32 v71, v110, v111\n\t"
  "v_cndmask_b32 v68, v53, v68, s[58:59]\n\t"
  "v_cndmask_b32 v69, v53, v69, s[58:59]\n\t"
  "v_cndmask_b32 v70, v53, v70, s[58:59]\n\t"
  "v_cndmask_b32 v71, v53, v71, s[58:59]\n\t"
  "buffer_store_dwordx4 v[68:71], v39, s[36:39], s45 offen offset:1024 sc1\n\t"
  "v_cvt_pk_bf16_f32 v72, v112, v113\n\t"
  "v_cvt_pk_bf16_f32 v73, v114, v115\n\t"
  "v_cvt_pk_bf16_f32 v74, v116, v117\n\t"
  "v_cvt_pk_bf16_f32 v75, v118, v119\n\t"
  "v_cndmask_b32 v72, v53, v72, s[58:59]\n\t"
  "v_cndmask_b32 v73, v53, v73, s[58:59]\n\t"
  "v_cndmask_b32 v74, v53, v74, s[58:59]\n\t"
  "v_cndmask_b32 v75, v53, v75, s[58:59]\n\t"
  "buffer_store_dwordx4 v[72:75], v39, s[36:39], s46 offen offset:0 sc1\n\t"
  "v_cvt_pk_bf16_f32 v76, v120, v121\n\t"
  "v_cvt_pk_bf16_f32 v77, v122, v123\n\t"
  "v_cvt_pk_bf16_f32 v78, v124, v125\n\t"
  "v_cvt_pk_bf16_f32 v79, v126, v127\n\t"
  "v_cndmask_b32 v76, v53, v76, s[58:59]\n\t"
  "v_cndmask_b32 v77, v53, v77, s[58:59]\n\t"
  "v_cndmask_b32 v78, v53, v78, s[58:59]\n\t"
  "v_cndmask_b32 v79, v53, v79, s[58:59]\n\t"
  "buffer_store_dwordx4 v[76:79], v39, s[36:39], s46 offen offset:1024 sc1\n\t"
  "s_barrier\n\t"
  "ds_read_b64 v[56:57], v54 offset:96\n\t"
  "v_max3_f32 v12, |v96|, |v97|, |v98|\n\t"
  "v_max3_f32 v13, |v112|, |v113|, |v114|\n\t"
  "v_max3_f32 v12, v12, |v99|, |v100|\n\t"
  "v_max3_f32 v13, v13, |v115|, |v116|\n\t"
  "v_max3_f32 v12, v12, |v101|, |v102|\n\t"
  "v_max3_f32 v13, v13, |v117|, |v118|\n\t"
  "v_max3_f32 v12, v12, |v103|, |v104|\n\t"
  "v_max3_f32 v13, v13, |v119|, |v120|\n\t"
  "v_max3_f32 v12, v12, |v105|, |v106|\n\t"
  "v_max3_f32 v13, v13, |v121|, |v122|\n\t"
  "v_max3_f32 v12, v12, |v107|, |v108|\n\t"
  "v_max3_f32 v13, v13, |v123|, |v124|\n\t"
  "v_max3_f32 v12, v12, |v109|, |v110|\n\t"
  "v_max3_f32 v13, v13, |v125|, |v126|\n\t"
  "v_max_f32_e64 v12, v12, |v111|\n\t"
  "v_max_f32_e64 v13, v13, |v127|\n\t"
  "v_cndmask_b32 v12, 0, v12, s[58:59]\n\t"
  "v_cndmask_b32 v13, 0, v13, s[58:59]\n\t"
  "s_nop 1\n\t"
  "v_max_u32_dpp v12, v12, v12 row_shr:1\n\t"
  "v_max_u32_dpp v13, v13, v13 row_shr:1\n\t"
  "s_nop 0\n\t"
  "v_max_u32_dpp v12, v12, v12 row_shr:2\n\t"
  "v_max_u32_dpp v13, v13, v13 row_shr:2\n\t"
  "s_nop 0\n\t"
  "v_max_u32_dpp v12, v12, v12 row_shr:4\n\t"
  "v_max_u32_dpp v13, v13, v13 row_shr:4\n\t"
  "s_nop 0\n\t"
  "v_max_u32_dpp v12, v12, v12 row_shr:8\n\t"
  "v_max_u32_dpp v13, v13, v13 row_shr:8\n\t"
  "s_nop 0\n\t"
  "v_max_u32_dpp v12, v12, v12 row_bcast:15 row_mask:0xa\n\t"
  "v_max_u32_dpp v13, v13, v13 row_bcast:15 row_mask:0xa\n\t"
  "s_nop 0\n\t"
  "v_max_u32_dpp v12, v12, v12 row_bcast:31 row_mask:0xc\n\t"
  "v_max_u32_dpp v13, v13, v13 row_bcast:31 row_mask:0xc\n\t"
  "s_nop 0\n\t"
  "s_mov_b32 exec_lo, 0\n\t"
  "s_brev_b32 exec_hi, 1\n\t"
  "ds_max_u32 v54, v12 offset:64\n\t"
  "ds_max_u32 v54, v13 offset:68\n\t"
  "s_mov_b64 exec, -1\n\t"
  "LJ_%=:\n\t"
  "s_mov_b32 s51, 0\n\t"
  "s_waitcnt vmcnt(0) lgkmcnt(0)\n\t"
  "s_mov_b64 exec, 1\n\t"
  "ds_add_rtn_u32 v63, v54, v55 offset:40\n\t"
  "s_mov_b64 exec, -1\n\t"
  "s_waitcnt lgkmcnt(0)\n\t"
  "v_readfirstlane_b32 s52, v63\n\t"
  "s_nop 0\n\t"
  "s_cmp_lg_u32 s52, 3\n\t"
  "s_cbranch_scc1 LN0_%=\n\t"
  "buffer_store_dwordx2 v[56:57], v40, s[36:39], 0 offen sc1\n\t"
  "ds_read_b64 v[12:13], v54 offset:64\n\t"
  "s_mov_b32 s51, 1\n\t"
  "s_waitcnt lgkmcnt(0)\n\t"
  "s_mov_b64 vcc, 2\n\t"
  "v_mov_b32 v15, v56\n\t"
  "v_cndmask_b32 v14, v12, v13, vcc\n\t"
  "buffer_store_dwordx2 v[14:15], v47, s[36:39], 0 offen sc1\n\t"
  "LN0_%=:\n\t"
  "s_branch LWQ_%=\n\t"
  "LWQ_%=S:\n\t"
  "s_sleep 1\n\t"
  "LWQ_%=:\n\t"
  "ds_read_b32 v63, v54 offset:88\n\t"
  "s_waitcnt lgkmcnt(0)\n\t"
  "v_readfirstlane_b32 s52, v63\n\t"
  "s_nop 0\n\t"
  "s_cmp_lt_u32 s52, 4\n\t"
  "s_cbranch_scc1 LWQ_%=S\n\t"
  "ds_read_b64_tr_b16 v[80:81], v38 offset:0\n\t"
  "ds_read_b64_tr_b16 v[82:83], v38 offset:256\n\t"
  "ds_read_b64_tr_b16 v[84:85], v38 offset:1024\n\t"
  "ds_read_b64_tr_b16 v[86:87], v38 offset:1280\n\t"
  "ds_read_b64_tr_b16 v[88:89], v38 offset:2048\n\t"
  "ds_read_b64_tr_b16 v[90:91], v38 offset:2304\n\t"
  "s_waitcnt lgkmcnt(4)\n\t"
  "v_mfma_f32_32x32x16_bf16 v[128:143], v[80:83], v[144:147], 0\n\t"
  "ds_read_b64_tr_b16 v[92:93], v38 offset:3072\n\t"
  "ds_read_b64_tr_b16 v[94:95], v38 offset:3328\n\t"
  "s_waitcnt lgkmcnt(4)\n\t"
  "v_mfma_f32_32x32x16_bf16 v[128:143], v[84:87], v[148:151], v[128:143]\n\t"
  "ds_read_b64_tr_b16 v[80:81], v38 offset:4096\n\t"
  "ds_read_b64_tr_b16 v[82:83], v38 offset:4352\n\t"
  "s_waitcnt lgkmcnt(4)\n\t"
  "v_mfma_f32_32x32x16_bf16 v[128:143], v[88:91], v[152:155], v[128:143]\n\t"
  "ds_read_b64_tr_b16 v[84:85], v38 offset:5120\n\t"
  "ds_read_b64_tr_b16 v[86:87], v38 offset:5376\n\t"
  "s_waitcnt lgkmcnt(4)\n\t"
  "v_mfma_f32_32x32x16_bf16 v[128:143], v[92:95], v[156:159], v[128:143]\n\t"
  "ds_read_b64_tr_b16 v[88:89], v38 offset:6144\n\t"
  "ds_read_b64_tr_b16 v[90:91], v38 offset:6400\n\t"
  "s_waitcnt lgkmcnt(4)\n\t"
  "v_mfma_f32_32x32x16_bf16 v[128:143], v[80:83], v[160:163], v[128:143]\n\t"
  "ds_read_b64_tr_b16 v[92:93], v38 offset:7168\n\t"
  "ds_read_b64_tr_b16 v[94:95], v38 offset:7424\n\t"
  "s_waitcnt lgkmcnt(4)\n\t"
  "v_mfma_f32_32x32x16_bf16 v[128:143], v[84:87], v[164:167], v[128:143]\n\t"
  "ds_read_b64_tr_b16 v[80:81], v38 offset:8192\n\t"
  "ds_read_b64_tr_b16 v[82:83], v38 offset:8448\n\t"
  "s_waitcnt lgkmcnt(4)\n\t"
  "v_mfma_f32_32x32x16_bf16 v[128:143], v[88:91], v[168:171], v[128:143]\n\t"
  "ds_read_b64_tr_b16 v[84:85], v38 offset:9216\n\t"
  "ds_read_b64_tr_b16 v[86:87], v38 offset:9472\n\t"
  "s_waitcnt lgkmcnt(4)\n\t"
  "v_mfma_f32_32x32x16_bf16 v[128:143], v[92:95], v[172:175], v[128:143]\n\t"
  "ds_read_b64_tr_b16 v[88:89], v38 offset:10240\n\t"
  "ds_read_b64_tr_b16 v[90:91], v38 offset:10496\n\t"
  "s_waitcnt lgkmcnt(4)\n\t"
  "v_mfma_f32_32x32x16_bf16 v[128:143], v[80:83], v[176:179], v[128:143]\n\t"
  "ds_read_b64_tr_b16 v[92:93], v38 offset:11264\n\t"
  "ds_read_b64_tr_b16 v[94:95], v38 offset:11520\n\t"
  "s_waitcnt lgkmcnt(4)\n\t"
  "v_mfma_f32_32x32x16_bf16 v[128:143], v[84:87], v[180:183], v[128:143]\n\t"
  "ds_read_b64_tr_b16 v[80:81], v38 offset:12288\n\t"
  "ds_read_b64_tr_b16 v[82:83], v38 offset:12544\n\t"
  "s_waitcnt lgkmcnt(4)\n\t"
  "v_mfma_f32_32x32x16_bf16 v[128:143], v[88:91], v[184:187], v[128:143]\n\t"
  "ds_read_b64_tr_b16 v[84:85], v38 offset:13312\n\t"
  "ds_read_b64_tr_b16 v[86:87], v38 offset:13568\n\t"
  "s_waitcnt lgkmcnt(4)\n\t"
  "v_mfma_f32_32x32x16_bf16 v[128:143], v[92:95], v[188:191], v[128:143]\n\t"
  "ds_read_b64_tr_b16 v[88:89], v38 offset:14336\n\t"
  "ds_read_b64_tr_b16 v[90:91], v38 offset:14592\n\t"
  "s_waitcnt lgkmcnt(4)\n\t"
  "v_mfma_f32_32x32x16_bf16 v[128:143], v[80:83], v[192:195], v[128:143]\n\t"
  "ds_read_b64_tr_b16 v[92:93], v38 offset:15360\n\t"
  "ds_read_b64_tr_b16 v[94:95], v38 offset:15616\n\t"
  "s_waitcnt lgkmcnt(4)\n\t"
  "v_mfma_f32_32x32x16_bf16 v[128:143], v[84:87], v[196:199], v[128:143]\n\t"
  "ds_read_b64_tr_b16 v[80:81], v38 offset:16384\n\t"
  "ds_read_b64_tr_b16 v[82:83], v38 offset:16640\n\t"
  "s_waitcnt lgkmcnt(4)\n\t"
  "v_mfma_f32_32x32x16_bf16 v[128:143], v[88:91], v[200:203], v[128:143]\n\t"
  "ds_read_b64_tr_b16 v[84:85], v38 offset:17408\n\t"
  "ds_read_b64_tr_b16 v[86:87], v38 offset:17664\n\t"
  "s_waitcnt lgkmcnt(4)\n\t"
  "v_mfma_f32_32x32x16_bf16 v[128:143], v[92:95], v[204:207], v[128:143]\n\t"
  "ds_read_b64_tr_b16 v[88:89], v38 offset:18432\n\t"
  "ds_read_b64_tr_b16 v[90:91], v38 offset:18688\n\t"
  "s_waitcnt lgkmcnt(4)\n\t"
  "v_mfma_f32_32x32x16_bf16 v[128:143], v[80:83], v[208:211], v[128:143]\n\t"
  "ds_read_b64_tr_b16 v[92:93], v38 offset:19456\n\t"
  "ds_read_b64_tr_b16 v[94:95], v38 offset:19712\n\t"
  "s_waitcnt lgkmcnt(4)\n\t"
  "v_mfma_f32_32x32x16_bf16 v[128:143], v[84:87], v[212:215], v[128:143]\n\t"
  "ds_read_b64_tr_b16 v[80:81], v38 offset:20480\n\t"
  "ds_read_b64_tr_b16 v[82:83], v38 offset:20736\n\t"
  "s_waitcnt lgkmcnt(4)\n\t"
  "v_mfma_f32_32x32x16_bf16 v[128:143], v[88:91], v[216:219], v[128:143]\n\t"
  "ds_read_b64_tr_b16 v[84:85], v38 offset:21504\n\t"
  "ds_read_b64_tr_b16 v[86:87], v38 offset:21760\n\t"
  "s_waitcnt lgkmcnt(4)\n\t"
  "v_mfma_f32_32x32x16_bf16 v[128:143], v[92:95], v[220:223], v[128:143]\n\t"
  "ds_read_b64_tr_b16 v[88:89], v38 offset:22528\n\t"
  "ds_read_b64_tr_b16 v[90:91], v38 offset:22784\n\t"
  "s_waitcnt lgkmcnt(4)\n\t"
  "v_mfma_f32_32x32x16_bf16 v[128:143], v[80:83], v[224:227], v[128:143]\n\t"
  "ds_read_b64_tr_b16 v[92:93], v38 offset:23552\n\t"
  "ds_read_b64_tr_b16 v[94:95], v38 offset:23808\n\t"
  "s_waitcnt lgkmcnt(4)\n\t"
  "v_mfma_f32_32x32x16_bf16 v[128:143], v[84:87], v[228:231], v[128:143]\n\t"
  "ds_read_b64_tr_b16 v[80:81], v38 offset:24576\n\t"
  "ds_read_b64_tr_b16 v[82:83], v38 offset:24832\n\t"
  "s_waitcnt lgkmcnt(4)\n\t"
  "v_mfma_f32_32x32x16_bf16 v[128:143], v[88:91], v[232:235], v[128:143]\n\t"
  "ds_read_b64_tr_b16 v[84:85], v38 offset:25600\n\t"
  "ds_read_b64_tr_b16 v[86:87], v38 offset:25856\n\t"
  "s_waitcnt lgkmcnt(4)\n\t"
  "v_mfma_f32_32x32x16_bf16 v[128:143], v[92:95], v[236:239], v[128:143]\n\t"
  "ds_read_b64_tr_b16 v[88:89], v38 offset:26624\n\t"
  "ds_read_b64_tr_b16 v[90:91], v38 offset:26880\n\t"
  "s_waitcnt lgkmcnt(4)\n\t"
  "v_mfma_f32_32x32x16_bf16 v[128:143], v[80:83], v[240:243], v[128:143]\n\t"
  "ds_read_b64_tr_b16 v[92:93], v38 offset:27648\n\t"
  "ds_read_b64_tr_b16 v[94:95], v38 offset:27904\n\t"
  "s_waitcnt lgkmcnt(4)\n\t"
  "v_mfma_f32_32x32x16_bf16 v[128:143], v[84:87], v[244:247], v[128:143]\n\t"
  "s_waitcnt lgkmcnt(2)\n\t"
  "v_mfma_f32_32x32x16_bf16 v[128:143], v[88:91], v[248:251], v[128:143]\n\t"
  "s_waitcnt lgkmcnt(0)\n\t"
  "v_mfma_f32_32x32x16_bf16 v[128:143], v[92:95], v[252:255], v[128:143]\n\t"
  "s_nop 8\n\t"
  "s_mov_b32 s52, 0x3e8b97f5\n\t"
  "v_mov_b64 v[252:253], 0\n\t"
  "v_mov_b64 v[254:255], 0\n\t"
  "v_pk_mul_f32 v[128:129], v[128:129], s[52:53] op_sel_hi:[1,0]\n\t"
  "v_pk_mul_f32 v[130:131], v[130:131], s[52:53] op_sel_hi:[1,0]\n\t"
  "v_pk_mul_f32 v[132:133], v[132:133], s[52:53] op_sel_hi:[1,0]\n\t"
  "v_pk_mul_f32 v[134:135], v[134:135], s[52:53] op_sel_hi:[1,0]\n\t"
  "v_pk_mul_f32 v[136:137], v[136:137], s[52:53] op_sel_hi:[1,0]\n\t"
  "v_pk_mul_f32 v[138:139], v[138:139], s[52:53] op_sel_hi:[1,0]\n\t"
  "v_pk_mul_f32 v[140:141], v[140:141], s[52:53] op_sel_hi:[1,0]\n\t"
  "v_pk_mul_f32 v[142:143], v[142:143], s[52:53] op_sel_hi:[1,0]\n\t"
  "v_mfma_f32_32x32x16_bf16 v[204:219], v[252:255], v[252:255], 0\n\t"
  "v_cvt_pk_bf16_f32 v64, v128, v129\n\t"
  "v_cvt_pk_bf16_f32 v65, v130, v131\n\t"
  "v_mfma_f32_32x32x16_bf16 v[220:235], v[252:255], v[252:255], 0\n\t"
  "v_cvt_pk_bf16_f32 v66, v132, v133\n\t"
  "v_cvt_pk_bf16_f32 v67, v134, v135\n\t"
  "v_mfma_f32_32x32x16_bf16 v[236:251], v[252:255], v[252:255], 0\n\t"
  "v_cvt_pk_bf16_f32 v68, v136, v137\n\t"
  "v_cvt_pk_bf16_f32 v69, v138, v139\n\t"
  "v_mfma_f32_32x32x16_bf16 v[16:31], v[252:255], v[252:255], 0\n\t"
  "v_cvt_pk_bf16_f32 v70, v140, v141\n\t"
  "v_cvt_pk_bf16_f32 v71, v142, v143\n\t"
  "v_cndmask_b32 v70, 0, v70, s[60:61]\n\t"
  "v_cndmask_b32 v71, 0, v71, s[60:61]\n\t"
  "ds_write_b128 v48, v[64:67]\n\t"
  "ds_write_b128 v48, v[68:71] offset:1024\n\t"
  "s_waitcnt lgkmcnt(0)\n\t"
  "s_mov_b64 exec, 1\n\t"
  "ds_add_u32 v54, v55 offset:16\n\t"
  "s_mov_b64 exec, -1\n\t"
  "v_mul_f32 v8, v128, v128\n\t"
  "v_mul_f32 v9, v140, v140\n\t"
  "v_fmac_f32 v8, v129, v129\n\t"
  "v_fmac_f32 v9, v141, v141\n\t"
  "v_fmac_f32 v8, v130, v130\n\t"
  "v_fmac_f32 v9, v142, v142\n\t"
  "v_fmac_f32 v8, v131, v131\n\t"
  "v_fmac_f32 v9, v143, v143\n\t"
  "v_fmac_f32 v8, v132, v132\n\t"
  "v_fmac_f32 v8, v133, v133\n\t"
  "v_fmac_f32 v8, v134, v134\n\t"
  "v_fmac_f32 v8, v135, v135\n\t"
  "v_fmac_f32 v8, v136, v136\n\t"
  "v_fmac_f32 v8, v137, v137\n\t"
  "v_fmac_f32 v8, v138, v138\n\t"
  "v_fmac_f32 v8, v139, v139\n\t"
  "v_cndmask_b32 v9, 0, v9, s[60:61]\n\t"
  "v_add_f32 v8, v8, v9\n\t"
  "v_mov_b32 v12, v8\n\t"
  "s_nop 1\n\t"
  "v_permlane32_swap_b32 v8, v12\n\t"
  "s_nop 0\n\t"
  "v_add_f32 v8, v8, v12\n\t"
  "s_nop 1\n\t"
  "v_max_u32_dpp v8, v8, v8 row_shr:1\n\t"
  "s_nop 1\n\t"
  "v_max_u32_dpp v8, v8, v8 row_shr:2\n\t"
  "s_nop 1\n\t"
  "v_max_u32_dpp v8, v8, v8 row_shr:4\n\t"
  "s_nop 1\n\t"
  "v_max_u32_dpp v8, v8, v8 row_shr:8\n\t"
  "s_nop 1\n\t"
  "v_max_u32_dpp v8, v8, v8 row_bcast:15 row_mask:0xa\n\t"
  "s_nop 1\n\t"
  "v_max_u32_dpp v8, v8, v8 row_bcast:31 row_mask:0xc\n\t"
  "s_nop 1\n\t"
  "s_mov_b32 exec_lo, 0\n\t"
  "s_brev_b32 exec_hi, 1\n\t"
  "ds_max_u32 v54, v8 offset:4\n\t"
  "s_mov_b64 exec, -1\n\t"
  "v_mov_b64 v[136:137], 0\n\t"
  "v_mov_b64 v[138:139], 0\n\t"
  "v_mov_b64 v[128:129], 0\n\t"
  "v_mov_b64 v[130:131], 0\n\t"
  "s_cmp_eq_u32 s51, 0\n\t"
  "s_cbranch_scc1 LN1_%=\n\t"
  "s_mov_b32 s53, 0x20000\n\t"
  "LP1_%=:\n\t"
  "buffer_load_dwordx2 v[12:13], v41, s[36:39], 0 offen sc1\n\t"
  "s_sub_u32 s53, s53, 1\n\t"
  "s_waitcnt vmcnt(0)\n\t"
  "v_cmp_ne_u64 vcc, v[12:13], v[56:57]\n\t"
  "s_cbranch_scc1 LP1_%=E\n\t"
  "s_cbranch_vccnz LP1_%=\n\t"
  "LP1_%=E:\n\t"
  "s_waitcnt lgkmcnt(0)\n\t"
  "s_mov_b64 exec, 1\n\t"
  "ds_add_u32 v54, v55 offset:20\n\t"
  "s_mov_b64 exec, -1\n\t"
  "LN1_%=:\n\t"
  "s_branch LW1_%=\n\t"
  "LW1S_%=:\n\t"
  "s_sleep 1\n\t"
  "LW1_%=:\n\t"
  "ds_read_b64 v[12:13], v54 offset:16\n\t"
  "s_waitcnt lgkmcnt(0)\n\t"
  "v_add_u32 v12, v12, v13\n\t"
  "s_nop 0\n\t"
  "v_readfirstlane_b32 s52, v12\n\t"
  "s_nop 0\n\t"
  "s_cmp_lt_u32 s52, 5\n\t"
  "s_cbranch_scc1 LW1S_%=\n\t"
  "s_mov_b32 s54, s47\n\t"
  "buffer_load_dwordx4 v[140:143], v39, s[40:43], s54 offen offset:0 sc1\n\t"
  "buffer_load_dwordx4 v[144:147], v39, s[40:43], s54 offen offset:1024 sc1\n\t"
  "buffer_load_dwordx4 v[148:151], v39, s[40:43], s54 offen offset:2048 sc1\n\t"
  "buffer_load_dwordx4 v[152:155], v39, s[40:43], s54 offen offset:3072 sc1\n\t"
  "s_add_u32 s54, s54, 4096\n\t"
  "s_and_b32 s54, s54, 0xffff\n\t"
  "buffer_load_dwordx4 v[156:159], v39, s[40:43], s54 offen offset:0 sc1\n\t"
  "buffer_load_dwordx4 v[160:163], v39, s[40:43], s54 offen offset:1024 sc1\n\t"
  "buffer_load_dwordx4 v[164:167], v39, s[40:43], s54 offen offset:2048 sc1\n\t"
  "buffer_load_dwordx4 v[168:171], v39, s[40:43], s54 offen offset:3072 sc1\n\t"
  "s_add_u32 s54, s54, 4096\n\t"
  "s_and_b32 s54, s54, 0xffff\n\t"
  "buffer_load_dwordx4 v[172:175], v39, s[40:43], s54 offen offset:0 sc1\n\t"
  "buffer_load_dwordx4 v[176:179], v39, s[40:43], s54 offen offset:1024 sc1\n\t"
  "buffer_load_dwordx4 v[180:183], v39, s[40:43], s54 offen offset:2048 sc1\n\t"
  "buffer_load_dwordx4 v[184:187], v39, s[40:43], s54 offen offset:3072 sc1\n\t"
  "s_add_u32 s54, s54, 4096\n\t"
  "s_and_b32 s54, s54, 0xffff\n\t"
  "ds_read_b128 v[64:67], v49 offset:0\n\t"
  "ds_read_b128 v[68:71], v49 offset:1024\n\t"
  "ds_read_b128 v[72:75], v50 offset:0\n\t"
  "ds_read_b128 v[76:79], v50 offset:1024\n\t"
  "ds_read_b128 v[80:83], v51 offset:0\n\t"
  "ds_read_b128 v[84:87], v51 offset:1024\n\t"
  "ds_read_b128 v[88:91], v52 offset:0\n\t"
  "ds_read_b128 v[92:95], v52 offset:1024\n\t"
  "s_mov_b32 s55, 2\n\t"
  "s_waitcnt vmcnt(10) lgkmcnt(0)\n\t"
  "v_mfma_f32_32x32x16_bf16 v[96:111], v[140:143], v[64:67], 0\n\t"
  "v_mfma_f32_32x32x16_bf16 v[96:111], v[144:147], v[68:71], v[96:111]\n\t"
  "s_nop 13\n\t"
  "v_mfma_f32_32x32x16_bf16 v[112:127], v[140:143], v[72:75], 0\n\t"
  "v_exp_f32 v128, v96\n\t"
  "v_exp_f32 v129, v97\n\t"
  "v_cvt_pk_bf16_f32 v139, v130, v131\n\t"
  "v_exp_f32 v130, v98\n\t"
  "v_exp_f32 v131, v99\n\t"
  "v_cvt_pk_bf16_f32 v132, v128, v129\n\t"
  "v_mfma_f32_32x32x16_bf16 v[16:31], v[252:255], v[136:139], v[16:31]\n\t"
  "v_exp_f32 v128, v100\n\t"
  "v_exp_f32 v129, v101\n\t"
  "v_cvt_pk_bf16_f32 v133, v130, v131\n\t"
  "v_exp_f32 v130, v102\n\t"
  "v_exp_f32 v131, v103\n\t"
  "v_cvt_pk_bf16_f32 v134, v128, v129\n\t"
  "v_mfma_f32_32x32x16_bf16 v[112:127], v[144:147], v[76:79], v[112:127]\n\t"
  "s_waitcnt vmcnt(8)\n\t"
  "v_exp_f32 v128, v104\n\t"
  "v_exp_f32 v129, v105\n\t"
  "v_cvt_pk_bf16_f32 v135, v130, v131\n\t"
  "v_exp_f32 v130, v106\n\t"
  "v_exp_f32 v131, v107\n\t"
  "v_cvt_pk_bf16_f32 v136, v128, v129\n\t"
  "v_mfma_f32_32x32x16_bf16 v[204:219], v[148:151], v[132:135], v[204:219]\n\t"
  "v_exp_f32 v128, v108\n\t"
  "v_exp_f32 v129, v109\n\t"
  "v_cvt_pk_bf16_f32 v137, v130, v131\n\t"
  "v_exp_f32 v130, v110\n\t"
  "v_exp_f32 v131, v111\n\t"
  "v_cvt_pk_bf16_f32 v138, v128, v129\n\t"
  "v_mfma_f32_32x32x16_bf16 v[96:111], v[140:143], v[80:83], 0\n\t"
  "buffer_load_dwordx4 v[188:191], v39, s[40:43], s54 offen offset:0 sc1\n\t"
  "v_exp_f32 v128, v112\n\t"
  "v_exp_f32 v129, v113\n\t"
  "v_cvt_pk_bf16_f32 v139, v130, v131\n\t"
  "v_exp_f32 v130, v114\n\t"
  "v_exp_f32 v131, v115\n\t"
  "v_cvt_pk_bf16_f32 v132, v128, v129\n\t"
  "v_mfma_f32_32x32x16_bf16 v[204:219], v[152:155], v[136:139], v[204:219]\n\t"
  "buffer_load_dwordx4 v[192:195], v39, s[40:43], s54 offen offset:1024 sc1\n\t"
  "v_exp_f32 v128, v116\n\t"
  "v_exp_f32 v129, v117\n\t"
  "v_cvt_pk_bf16_f32 v133, v130, v131\n\t"
  "v_exp_f32 v130, v118\n\t"
  "v_exp_f32 v131, v119\n\t"
  "v_cvt_pk_bf16_f32 v134, v128, v129\n\t"
  "v_mfma_f32_32x32x16_bf16 v[96:111], v[144:147], v[84:87], v[96:111]\n\t"
  "buffer_load_dwordx4 v[196:199], v39, s[40:43], s54 offen offset:2048 sc1\n\t"
  "v_exp_f32 v128, v120\n\t"
  "v_exp_f32 v129, v121\n\t"
  "v_cvt_pk_bf16_f32 v135, v130, v131\n\t"
  "v_exp_f32 v130, v122\n\t"
  "v_exp_f32 v131, v123\n\t"
  "v_cvt_pk_bf16_f32 v136, v128, v129\n\t"
  "v_mfma_f32_32x32x16_bf16 v[220:235], v[148:151], v[132:135], v[220:235]\n\t"
  "buffer_load_dwordx4 v[200:203], v39, s[40:43], s54 offen offset:3072 sc1\n\t"
  "v_exp_f32 v128, v124\n\t"
  "v_exp_f32 v129, v125\n\t"
  "v_cvt_pk_bf16_f32 v137, v130, v131\n\t"
  "v_exp_f32 v130, v126\n\t"
  "v_exp_f32 v131, v127\n\t"
  "v_cvt_pk_bf16_f32 v138, v128, v129\n\t"
  "v_mfma_f32_32x32x16_bf16 v[112:127], v[140:143], v[88:91], 0\n\t"
  "s_add_u32 s54, s54, 4096\n\t"
  "v_exp_f32 v128, v96\n\t"
  "v_exp_f32 v129, v97\n\t"
  "v_cvt_pk_bf16_f32 v139, v130, v131\n\t"
  "v_exp_f32 v130, v98\n\t"
  "v_exp_f32 v131, v99\n\t"
  "v_cvt_pk_bf16_f32 v132, v128, v129\n\t"
  "v_mfma_f32_32x32x16_bf16 v[220:235], v[152:155], v[136:139], v[220:235]\n\t"
  "s_and_b32 s54, s54, 0xffff\n\t"
  "v_exp_f32 v128, v100\n\t"
  "v_exp_f32 v129, v101\n\t"
  "v_cvt_pk_bf16_f32 v133, v130, v131\n\t"
  "v_exp_f32 v130, v102\n\t"
  "v_exp_f32 v131, v103\n\t"
  "v_cvt_pk_bf16_f32 v134, v128, v129\n\t"
  "v_mfma_f32_32x32x16_bf16 v[112:127], v[144:147], v[92:95], v[112:127]\n\t"
  "v_exp_f32 v128, v104\n\t"
  "v_exp_f32 v129, v105\n\t"
  "v_cvt_pk_bf16_f32 v135, v130, v131\n\t"
  "v_exp_f32 v130, v106\n\t"
  "v_exp_f32 v131, v107\n\t"
  "v_cvt_pk_bf16_f32 v136, v128, v129\n\t"
  "v_mfma_f32_32x32x16_bf16 v[236:251], v[148:151], v[132:135], v[236:251]\n\t"
  "s_waitcnt vmcnt(8)\n\t"
  "v_exp_f32 v128, v108\n\t"
  "v_exp_f32 v129, v109\n\t"
  "v_cvt_pk_bf16_f32 v137, v130, v131\n\t"
  "v_exp_f32 v130, v110\n\t"
  "v_exp_f32 v131, v111\n\t"
  "v_cvt_pk_bf16_f32 v138, v128, v129\n\t"
  "v_mfma_f32_32x32x16_bf16 v[96:111], v[156:159], v[64:67], 0\n\t"
  "buffer_load_dwordx4 v[48:51], v39, s[40:43], s54 offen offset:0 sc1\n\t"
  "v_exp_f32 v128, v112\n\t"
  "v_exp_f32 v129, v113\n\t"
  "v_cvt_pk_bf16_f32 v139, v130, v131\n\t"
  "v_exp_f32 v130, v114\n\t"
  "v_exp_f32 v131, v115\n\t"
  "v_cvt_pk_bf16_f32 v132, v128, v129\n\t"
  "v_mfma_f32_32x32x16_bf16 v[236:251], v[152:155], v[136:139], v[236:251]\n\t"
  "buffer_load_dwordx4 v[32:35], v39, s[40:43], s54 offen offset:1024 sc1\n\t"
  "v_exp_f32 v128, v116\n\t"
  "v_exp_f32 v129, v117\n\t"
  "v_cvt_pk_bf16_f32 v133, v130, v131\n\t"
  "v_exp_f32 v130, v118\n\t"
  "v_exp_f32 v131, v119\n\t"
  "v_cvt_pk_bf16_f32 v134, v128, v129\n\t"
  "v_mfma_f32_32x32x16_bf16 v[96:111], v[160:163], v[68:71], v[96:111]\n\t"
  "buffer_load_dwordx4 v[58:61], v39, s[40:43], s54 offen offset:2048 sc1\n\t"
  "v_exp_f32 v128, v120\n\t"
  "v_exp_f32 v129, v121\n\t"
  "v_cvt_pk_bf16_f32 v135, v130, v131\n\t"
  "v_exp_f32 v130, v122\n\t"
  "v_exp_f32 v131, v123\n\t"
  "v_cvt_pk_bf16_f32 v136, v128, v129\n\t"
  "v_mfma_f32_32x32x16_bf16 v[16:31], v[148:151], v[132:135], v[16:31]\n\t"
  "buffer_load_dwordx4 v[252:255], v39, s[40:43], s54 offen offset:3072 sc1\n\t"
  "s_add_u32 s54, s54, 4096\n\t"
  "s_and_b32 s54, s54, 0xffff\n\t"
  "v_exp_f32 v128, v124\n\t"
  "v_exp_f32 v129, v125\n\t"
  "v_cvt_pk_bf16_f32 v137, v130, v131\n\t"
  "v_exp_f32 v130, v126\n\t"
  "v_exp_f32 v131, v127\n\t"
  "v_cvt_pk_bf16_f32 v138, v128, v129\n\t"
  "LATT_%=:\n\t"
  "v_mfma_f32_32x32x16_bf16 v[112:127], v[156:159], v[72:75], 0\n\t"
  "buffer_load_dwordx4 v[140:143], v39, s[40:43], s54 offen offset:0 sc1\n\t"
  "v_exp_f32 v128, v96\n\t"
  "v_exp_f32 v129, v97\n\t"
  "v_cvt_pk_bf16_f32 v139, v130, v131\n\t"
  "v_exp_f32 v130, v98\n\t"
  "v_exp_f32 v131, v99\n\t"
  "v_cvt_pk_bf16_f32 v132, v128, v129\n\t"
  "v_mfma_f32_32x32x16_bf16 v[16:31], v[152:155], v[136:139], v[16:31]\n\t"
  "v_exp_f32 v128, v100\n\t"
  "v_exp_f32 v129, v101\n\t"
  "v_cvt_pk_bf16_f32 v133, v130, v131\n\t"
  "v_exp_f32 v130, v102\n\t"
  "v_exp_f32 v131, v103\n\t"
  "v_cvt_pk_bf16_f32 v134, v128, v129\n\t"
  "v_mfma_f32_32x32x16_bf16 v[112:127], v[160:163], v[76:79], v[112:127]\n\t"
  "buffer_load_dwordx4 v[144:147], v39, s[40:43], s54 offen offset:1024 sc1\n\t"
  "v_exp_f32 v128, v104\n\t"
  "v_exp_f32 v129, v105\n\t"
  "v_cvt_pk_bf16_f32 v135, v130, v131\n\t"
  "v_exp_f32 v130, v106\n\t"
  "v_exp_f32 v131, v107\n\t"
  "v_cvt_pk_bf16_f32 v136, v128, v129\n\t"
  "v_mfma_f32_32x32x16_bf16 v[204:219], v[164:167], v[132:135], v[204:219]\n\t"
  "v_exp_f32 v128, v108\n\t"
  "v_exp_f32 v129, v109\n\t"
  "v_cvt_pk_bf16_f32 v137, v130, v131\n\t"
  "v_exp_f32 v130, v110\n\t"
  "v_exp_f32 v131, v111\n\t"
  "v_cvt_pk_bf16_f32 v138, v128, v129\n\t"
  "v_mfma_f32_32x32x16_bf16 v[96:111], v[156:159], v[80:83], 0\n\t"
  "buffer_load_dwordx4 v[148:151], v39, s[40:43], s54 offen offset:2048 sc1\n\t"
  "v_exp_f32 v128, v112\n\t"
  "v_exp_f32 v129, v113\n\t"
  "v_cvt_pk_bf16_f32 v139, v130, v131\n\t"
  "v_exp_f32 v130, v114\n\t"
  "v_exp_f32 v131, v115\n\t"
  "v_cvt_pk_bf16_f32 v132, v128, v129\n\t"
  "v_mfma_f32_32x32x16_bf16 v[204:219], v[168:171], v[136:139], v[204:219]\n\t"
  "v_exp_f32 v128, v116\n\t"
  "v_exp_f32 v129, v117\n\t"
  "v_cvt_pk_bf16_f32 v133, v130, v131\n\t"
  "v_exp_f32 v130, v118\n\t"
  "v_exp_f32 v131, v119\n\t"
  "v_cvt_pk_bf16_f32 v134, v128, v129\n\t"
  "v_mfma_f32_32x32x16_bf16 v[96:111], v[160:163], v[84:87], v[96:111]\n\t"
  "buffer_load_dwordx4 v[152:155], v39, s[40:43], s54 offen offset:3072 sc1\n\t"
  "v_exp_f32 v128, v120\n\t"
  "v_exp_f32 v129, v121\n\t"
  "v_cvt_pk_bf16_f32 v135, v130, v131\n\t"
  "v_exp_f32 v130, v122\n\t"
  "v_exp_f32 v131, v123\n\t"
  "v_cvt_pk_bf16_f32 v136, v128, v129\n\t"
  "v_mfma_f32_32x32x16_bf16 v[220:235], v[164:167], v[132:135], v[220:235]\n\t"
  "s_add_u32 s54, s54, 4096\n\t"
  "v_exp_f32 v128, v124\n\t"
  "v_exp_f32 v129, v125\n\t"
  "v_cvt_pk_bf16_f32 v137, v130, v131\n\t"
  "v_exp_f32 v130, v126\n\t"
  "v_exp_f32 v131, v127\n\t"
  "v_cvt_pk_bf16_f32 v138, v128, v129\n\t"
  "v_mfma_f32_32x32x16_bf16 v[112:127], v[156:159], v[88:91], 0\n\t"
  "s_and_b32 s54, s54, 0xffff\n\t"
  "v_exp_f32 v128, v96\n\t"
  "v_exp_f32 v129, v97\n\t"
  "v_cvt_pk_bf16_f32 v139, v130, v131\n\t"
  "v_exp_f32 v130, v98\n\t"
  "v_exp_f32 v131, v99\n\t"
  "v_cvt_pk_bf16_f32 v132, v128, v129\n\t"
  "v_mfma_f32_32x32x16_bf16 v[220:235], v[168:171], v[136:139], v[220:235]\n\t"
  "v_exp_f32 v128, v100\n\t"
  "v_exp_f32 v129, v101\n\t"
  "v_cvt_pk_bf16_f32 v133, v130, v131\n\t"
  "v_exp_f32 v130, v102\n\t"
  "v_exp_f32 v131, v103\n\t"
  "v_cvt_pk_bf16_f32 v134, v128, v129\n\t"
  "v_mfma_f32_32x32x16_bf16 v[112:127], v[160:163], v[92:95], v[112:127]\n\t"
  "v_exp_f32 v128, v104\n\t"
  "v_exp_f32 v129, v105\n\t"
  "v_cvt_pk_bf16_f32 v135, v130, v131\n\t"
  "v_exp_f32 v130, v106\n\t"
  "v_exp_f32 v131, v107\n\t"
  "v_cvt_pk_bf16_f32 v136, v128, v129\n\t"
  "v_mfma_f32_32x32x16_bf16 v[236:251], v[164:167], v[132:135], v[236:251]\n\t"
  "s_waitcnt vmcnt(12)\n\t"
  "v_exp_f32 v128, v108\n\t"
  "v_exp_f32 v129, v109\n\t"
  "v_cvt_pk_bf16_f32 v137, v130, v131\n\t"
  "v_exp_f32 v130, v110\n\t"
  "v_exp_f32 v131, v111\n\t"
  "v_cvt_pk_bf16_f32 v138, v128, v129\n\t"
  "v_mfma_f32_32x32x16_bf16 v[96:111], v[172:175], v[64:67], 0\n\t"
  "v_exp_f32 v128, v112\n\t"
  "v_exp_f32 v129, v113\n\t"
  "v_cvt_pk_bf16_f32 v139, v130, v131\n\t"
  "v_exp_f32 v130, v114\n\t"
  "v_exp_f32 v131, v115\n\t"
  "v_cvt_pk_bf16_f32 v132, v128, v129\n\t"
  "v_mfma_f32_32x32x16_bf16 v[236:251], v[168:171], v[136:139], v[236:251]\n\t"
  "v_exp_f32 v128, v116\n\t"
  "v_exp_f32 v129, v117\n\t"
  "v_cvt_pk_bf16_f32 v133, v130, v131\n\t"
  "v_exp_f32 v130, v118\n\t"
  "v_exp_f32 v131, v119\n\t"
  "v_cvt_pk_bf16_f32 v134, v128, v129\n\t"
  "v_mfma_f32_32x32x16_bf16 v[96:111], v[176:179], v[68:71], v[96:111]\n\t"
  "v_exp_f32 v128, v120\n\t"
  "v_exp_f32 v129, v121\n\t"
  "v_cvt_pk_bf16_f32 v135, v130, v131\n\t"
  "v_exp_f32 v130, v122\n\t"
  "v_exp_f32 v131, v123\n\t"
  "v_cvt_pk_bf16_f32 v136, v128, v129\n\t"
  "v_mfma_f32_32x32x16_bf16 v[16:31], v[164:167], v[132:135], v[16:31]\n\t"
  "v_exp_f32 v128, v124\n\t"
  "v_exp_f32 v129, v125\n\t"
  "v_cvt_pk_bf16_f32 v137, v130, v131\n\t"
  "v_exp_f32 v130, v126\n\t"
  "v_exp_f32 v131, v127\n\t"
  "v_cvt_pk_bf16_f32 v138, v128, v129\n\t"
  "v_mfma_f32_32x32x16_bf16 v[112:127], v[172:175], v[72:75], 0\n\t"
  "buffer_load_dwordx4 v[156:159], v39, s[40:43], s54 offen offset:0 sc1\n\t"
  "v_exp_f32 v128, v96\n\t"
  "v_exp_f32 v129, v97\n\t"
  "v_cvt_pk_bf16_f32 v139, v130, v131\n\t"
  "v_exp_f32 v130, v98\n\t"
  "v_exp_f32 v131, v99\n\t"
  "v_cvt_pk_bf16_f32 v132, v128, v129\n\t"
  "v_mfma_f32_32x32x16_bf16 v[16:31], v[168:171], v[136:139], v[16:31]\n\t"
  "v_exp_f32 v128, v100\n\t"
  "v_exp_f32 v129, v101\n\t"
  "v_cvt_pk_bf16_f32 v133, v130, v131\n\t"
  "v_exp_f32 v130, v102\n\t"
  "v_exp_f32 v131, v103\n\t"
  "v_cvt_pk_bf16_f32 v134, v128, v129\n\t"
  "v_mfma_f32_32x32x16_bf16 v[112:127], v[176:179], v[76:79], v[112:127]\n\t"
  "buffer_load_dwordx4 v[160:163], v39, s[40:43], s54 offen offset:1024 sc1\n\t"
  "v_exp_f32 v128, v104\n\t"
  "v_exp_f32 v129, v105\n\t"
  "v_cvt_pk_bf16_f32 v135, v130, v131\n\t"
  "v_exp_f32 v130, v106\n\t"
  "v_exp_f32 v131, v107\n\t"
  "v_cvt_pk_bf16_f32 v136, v128, v129\n\t"
  "v_mfma_f32_32x32x16_bf16 v[204:219], v[180:183], v[132:135], v[204:219]\n\t"
  "v_exp_f32 v128, v108\n\t"
  "v_exp_f32 v129, v109\n\t"
  "v_cvt_pk_bf16_f32 v137, v130, v131\n\t"
  "v_exp_f32 v130, v110\n\t"
  "v_exp_f32 v131, v111\n\t"
  "v_cvt_pk_bf16_f32 v138, v128, v129\n\t"
  "v_mfma_f32_32x32x16_bf16 v[96:111], v[172:175], v[80:83], 0\n\t"
  "buffer_load_dwordx4 v[164:167], v39, s[40:43], s54 offen offset:2048 sc1\n\t"
  "v_exp_f32 v128, v112\n\t"
  "v_exp_f32 v129, v113\n\t"
  "v_cvt_pk_bf16_f32 v139, v130, v131\n\t"
  "v_exp_f32 v130, v114\n\t"
  "v_exp_f32 v131, v115\n\t"
  "v_cvt_pk_bf16_f32 v132, v128, v129\n\t"
  "v_mfma_f32_32x32x16_bf16 v[204:219], v[184:187], v[136:139], v[204:219]\n\t"
  "v_exp_f32 v128, v116\n\t"
  "v_exp_f32 v129, v117\n\t"
  "v_cvt_pk_bf16_f32 v133, v130, v131\n\t"
  "v_exp_f32 v130, v118\n\t"
  "v_exp_f32 v131, v119\n\t"
  "v_cvt_pk_bf16_f32 v134, v128, v129\n\t"
  "v_mfma_f32_32x32x16_bf16 v[96:111], v[176:179], v[84:87], v[96:111]\n\t"
  "buffer_load_dwordx4 v[168:171], v39, s[40:43], s54 offen offset:3072 sc1\n\t"
  "v_exp_f32 v128, v120\n\t"
  "v_exp_f32 v129, v121\n\t"
  "v_cvt_pk_bf16_f32 v135, v130, v131\n\t"
  "v_exp_f32 v130, v122\n\t"
  "v_exp_f32 v131, v123\n\t"
  "v_cvt_pk_bf16_f32 v136, v128, v129\n\t"
  "v_mfma_f32_32x32x16_bf16 v[220:235], v[180:183], v[132:135], v[220:235]\n\t"
  "s_add_u32 s54, s54, 4096\n\t"
  "v_exp_f32 v128, v124\n\t"
  "v_exp_f32 v129, v125\n\t"
  "v_cvt_pk_bf16_f32 v137, v130, v131\n\t"
  "v_exp_f32 v130, v126\n\t"
  "v_exp_f32 v131, v127\n\t"
  "v_cvt_pk_bf16_f32 v138, v128, v129\n\t"
  "v_mfma_f32_32x32x16_bf16 v[112:127], v[172:175], v[88:91], 0\n\t"
  "s_and_b32 s54, s54, 0xffff\n\t"
  "v_exp_f32 v128, v96\n\t"
  "v_exp_f32 v129, v97\n\t"
  "v_cvt_pk_bf16_f32 v139, v130, v131\n\t"
  "v_exp_f32 v130, v98\n\t"
  "v_exp_f32 v131, v99\n\t"
  "v_cvt_pk_bf16_f32 v132, v128, v129\n\t"
  "v_mfma_f32_32x32x16_bf16 v[220:235], v[184:187], v[136:139], v[220:235]\n\t"
  "v_exp_f32 v128, v100\n\t"
  "v_exp_f32 v129, v101\n\t"
  "v_cvt_pk_bf16_f32 v133, v130, v131\n\t"
  "v_exp_f32 v130, v102\n\t"
  "v_exp_f32 v131, v103\n\t"
  "v_cvt_pk_bf16_f32 v134, v128, v129\n\t"
  "v_mfma_f32_32x32x16_bf16 v[112:127], v[176:179], v[92:95], v[112:127]\n\t"
  "v_exp_f32 v128, v104\n\t"
  "v_exp_f32 v129, v105\n\t"
  "v_cvt_pk_bf16_f32 v135, v130, v131\n\t"
  "v_exp_f32 v130, v106\n\t"
  "v_exp_f32 v131, v107\n\t"
  "v_cvt_pk_bf16_f32 v136, v128, v129\n\t"
  "v_mfma_f32_32x32x16_bf16 v[236:251], v[180:183], v[132:135], v[236:251]\n\t"
  "s_waitcnt vmcnt(12)\n\t"
  "v_exp_f32 v128, v108\n\t"
  "v_exp_f32 v129, v109\n\t"
  "v_cvt_pk_bf16_f32 v137, v130, v131\n\t"
  "v_exp_f32 v130, v110\n\t"
  "v_exp_f32 v131, v111\n\t"
  "v_cvt_pk_bf16_f32 v138, v128, v129\n\t"
  "v_mfma_f32_32x32x16_bf16 v[96:111], v[188:191], v[64:67], 0\n\t"
  "v_exp_f32 v128, v112\n\t"
  "v_exp_f32 v129, v113\n\t"
  "v_cvt_pk_bf16_f32 v139, v130, v131\n\t"
  "v_exp_f32 v130, v114\n\t"
  "v_exp_f32 v131, v115\n\t"
  "v_cvt_pk_bf16_f32 v132, v128, v129\n\t"
  "v_mfma_f32_32x32x16_bf16 v[236:251], v[184:187], v[136:139], v[236:251]\n\t"
  "v_exp_f32 v128, v116\n\t"
  "v_exp_f32 v129, v117\n\t"
  "v_cvt_pk_bf16_f32 v133, v130, v131\n\t"
  "v_exp_f32 v130, v118\n\t"
  "v_exp_f32 v131, v119\n\t"
  "v_cvt_pk_bf16_f32 v134, v128, v129\n\t"
  "v_mfma_f32_32x32x16_bf16 v[96:111], v[192:195], v[68:71], v[96:111]\n\t"
  "v_exp_f32 v128, v120\n\t"
  "v_exp_f32 v129, v121\n\t"
  "v_cvt_pk_bf16_f32 v135, v130, v131\n\t"
  "v_exp_f32 v130, v122\n\t"
  "v_exp_f32 v131, v123\n\t"
  "v_cvt_pk_bf16_f32 v136, v128, v129\n\t"
  "v_mfma_f32_32x32x16_bf16 v[16:31], v[180:183], v[132:135], v[16:31]\n\t"
  "v_exp_f32 v128, v124\n\t"
  "v_exp_f32 v129, v125\n\t"
  "v_cvt_pk_bf16_f32 v137, v130, v131\n\t"
  "v_exp_f32 v130, v126\n\t"
  "v_exp_f32 v131, v127\n\t"
  "v_cvt_pk_bf16_f32 v138, v128, v129\n\t"
  "v_mfma_f32_32x32x16_bf16 v[112:127], v[188:191], v[72:75], 0\n\t"
  "buffer_load_dwordx4 v[172:175], v39, s[40:43], s54 offen offset:0 sc1\n\t"
  "v_exp_f32 v128, v96\n\t"
  "v_exp_f32 v129, v97\n\t"
  "v_cvt_pk_bf16_f32 v139, v130, v131\n\t"
  "v_exp_f32 v130, v98\n\t"
  "v_exp_f32 v131, v99\n\t"
  "v_cvt_pk_bf16_f32 v132, v128, v129\n\t"
  "v_mfma_f32_32x32x16_bf16 v[16:31], v[184:187], v[136:139], v[16:31]\n\t"
  "v_exp_f32 v128, v100\n\t"
  "v_exp_f32 v129, v101\n\t"
  "v_cvt_pk_bf16_f32 v133, v130, v131\n\t"
  "v_exp_f32 v130, v102\n\t"
  "v_exp_f32 v131, v103\n\t"
  "v_cvt_pk_bf16_f32 v134, v128, v129\n\t"
  "v_mfma_f32_32x32x16_bf16 v[112:127], v[192:195], v[76:79], v[112:127]\n\t"
  "buffer_load_dwordx4 v[176:179], v39, s[40:43], s54 offen offset:1024 sc1\n\t"
  "v_exp_f32 v128, v104\n\t"
  "v_exp_f32 v129, v105\n\t"
  "v_cvt_pk_bf16_f32 v135, v130, v131\n\t"
  "v_exp_f32 v130, v106\n\t"
  "v_exp_f32 v131, v107\n\t"
  "v_cvt_pk_bf16_f32 v136, v128, v129\n\t"
  "v_mfma_f32_32x32x16_bf16 v[204:219], v[196:199], v[132:135], v[204:219]\n\t"
  "v_exp_f32 v128, v108\n\t"
  "v_exp_f32 v129, v109\n\t"
  "v_cvt_pk_bf16_f32 v137, v130, v131\n\t"
  "v_exp_f32 v130, v110\n\t"
  "v_exp_f32 v131, v111\n\t"
  "v_cvt_pk_bf16_f32 v138, v128, v129\n\t"
  "v_mfma_f32_32x32x16_bf16 v[96:111], v[188:191], v[80:83], 0\n\t"
  "buffer_load_dwordx4 v[180:183], v39, s[40:43], s54 offen offset:2048 sc1\n\t"
  "v_exp_f32 v128, v112\n\t"
  "v_exp_f32 v129, v113\n\t"
  "v_cvt_pk_bf16_f32 v139, v130, v131\n\t"
  "v_exp_f32 v130, v114\n\t"
  "v_exp_f32 v131, v115\n\t"
  "v_cvt_pk_bf16_f32 v132, v128, v129\n\t"
  "v_mfma_f32_32x32x16_bf16 v[204:219], v[200:203], v[136:139], v[204:219]\n\t"
  "v_exp_f32 v128, v116\n\t"
  "v_exp_f32 v129, v117\n\t"
  "v_cvt_pk_bf16_f32 v133, v130, v131\n\t"
  "v_exp_f32 v130, v118\n\t"
  "v_exp_f32 v131, v119\n\t"
  "v_cvt_pk_bf16_f32 v134, v128, v129\n\t"
  "v_mfma_f32_32x32x16_bf16 v[96:111], v[192:195], v[84:87], v[96:111]\n\t"
  "buffer_load_dwordx4 v[184:187], v39, s[40:43], s54 offen offset:3072 sc1\n\t"
  "v_exp_f32 v128, v120\n\t"
  "v_exp_f32 v129, v121\n\t"
  "v_cvt_pk_bf16_f32 v135, v130, v131\n\t"
  "v_exp_f32 v130, v122\n\t"
  "v_exp_f32 v131, v123\n\t"
  "v_cvt_pk_bf16_f32 v136, v128, v129\n\t"
  "v_mfma_f32_32x32x16_bf16 v[220:235], v[196:199], v[132:135], v[220:235]\n\t"
  "s_add_u32 s54, s54, 4096\n\t"
  "v_exp_f32 v128, v124\n\t"
  "v_exp_f32 v129, v125\n\t"
  "v_cvt_pk_bf16_f32 v137, v130, v131\n\t"
  "v_exp_f32 v130, v126\n\t"
  "v_exp_f32 v131, v127\n\t"
  "v_cvt_pk_bf16_f32 v138, v128, v129\n\t"
  "v_mfma_f32_32x32x16_bf16 v[112:127], v[188:191], v[88:91], 0\n\t"
  "s_and_b32 s54, s54, 0xffff\n\t"
  "v_exp_f32 v128, v96\n\t"
  "v_exp_f32 v129, v97\n\t"
  "v_cvt_pk_bf16_f32 v139, v130, v131\n\t"
  "v_exp_f32 v130, v98\n\t"
  "v_exp_f32 v131, v99\n\t"
  "v_cvt_pk_bf16_f32 v132, v128, v129\n\t"
  "v_mfma_f32_32x32x16_bf16 v[220:235], v[200:203], v[136:139], v[220:235]\n\t"
  "v_exp_f32 v128, v100\n\t"
  "v_exp_f32 v129, v101\n\t"
  "v_cvt_pk_bf16_f32 v133, v130, v131\n\t"
  "v_exp_f32 v130, v102\n\t"
  "v_exp_f32 v131, v103\n\t"
  "v_cvt_pk_bf16_f32 v134, v128, v129\n\t"
  "v_mfma_f32_32x32x16_bf16 v[112:127], v[192:195], v[92:95], v[112:127]\n\t"
  "v_exp_f32 v128, v104\n\t"
  "v_exp_f32 v129, v105\n\t"
  "v_cvt_pk_bf16_f32 v135, v130, v131\n\t"
  "v_exp_f32 v130, v106\n\t"
  "v_exp_f32 v131, v107\n\t"
  "v_cvt_pk_bf16_f32 v136, v128, v129\n\t"
  "v_mfma_f32_32x32x16_bf16 v[236:251], v[196:199], v[132:135], v[236:251]\n\t"
  "s_waitcnt vmcnt(12)\n\t"
  "v_exp_f32 v128, v108\n\t"
  "v_exp_f32 v129, v109\n\t"
  "v_cvt_pk_bf16_f32 v137, v130, v131\n\t"
  "v_exp_f32 v130, v110\n\t"
  "v_exp_f32 v131, v111\n\t"
  "v_cvt_pk_bf16_f32 v138, v128, v129\n\t"
  "v_mfma_f32_32x32x16_bf16 v[96:111], v[48:51], v[64:67], 0\n\t"
  "v_exp_f32 v128, v112\n\t"
  "v_exp_f32 v129, v113\n\t"
  "v_cvt_pk_bf16_f32 v139, v130, v131\n\t"
  "v_exp_f32 v130, v114\n\t"
  "v_exp_f32 v131, v115\n\t"
  "v_cvt_pk_bf16_f32 v132, v128, v129\n\t"
  "v_mfma_f32_32x32x16_bf16 v[236:251], v[200:203], v[136:139], v[236:251]\n\t"
  "v_exp_f32 v128, v116\n\t"
  "v_exp_f32 v129, v117\n\t"
  "v_cvt_pk_bf16_f32 v133, v130, v131\n\t"
  "v_exp_f32 v130, v118\n\t"
  "v_exp_f32 v131, v119\n\t"
  "v_cvt_pk_bf16_f32 v134, v128, v129\n\t"
  "v_mfma_f32_32x32x16_bf16 v[96:111], v[32:35], v[68:71], v[96:111]\n\t"
  "v_exp_f32 v128, v120\n\t"
  "v_exp_f32 v129, v121\n\t"
  "v_cvt_pk_bf16_f32 v135, v130, v131\n\t"
  "v_exp_f32 v130, v122\n\t"
  "v_exp_f32 v131, v123\n\t"
  "v_cvt_pk_bf16_f32 v136, v128, v129\n\t"
  "v_mfma_f32_32x32x16_bf16 v[16:31], v[196:199], v[132:135], v[16:31]\n\t"
  "v_exp_f32 v128, v124\n\t"
  "v_exp_f32 v129, v125\n\t"
  "v_cvt_pk_bf16_f32 v137, v130, v131\n\t"
  "v_exp_f32 v130, v126\n\t"
  "v_exp_f32 v131, v127\n\t"
  "v_cvt_pk_bf16_f32 v138, v128, v129\n\t"
  "v_mfma_f32_32x32x16_bf16 v[112:127], v[48:51], v[72:75], 0\n\t"
  "buffer_load_dwordx4 v[188:191], v39, s[40:43], s54 offen offset:0 sc1\n\t"
  "v_exp_f32 v128, v96\n\t"
  "v_exp_f32 v129, v97\n\t"
  "v_cvt_pk_bf16_f32 v139, v130, v131\n\t"
  "v_exp_f32 v130, v98\n\t"
  "v_exp_f32 v131, v99\n\t"
  "v_cvt_pk_bf16_f32 v132, v128, v129\n\t"
  "v_mfma_f32_32x32x16_bf16 v[16:31], v[200:203], v[136:139], v[16:31]\n\t"
  "v_exp_f32 v128, v100\n\t"
  "v_exp_f32 v129, v101\n\t"
  "v_cvt_pk_bf16_f32 v133, v130, v131\n\t"
  "v_exp_f32 v130, v102\n\t"
  "v_exp_f32 v131, v103\n\t"
  "v_cvt_pk_bf16_f32 v134, v128, v129\n\t"
  "v_mfma_f32_32x32x16_bf16 v[112:127], v[32:35], v[76:79], v[112:127]\n\t"
  "buffer_load_dwordx4 v[192:195], v39, s[40:43], s54 offen offset:1024 sc1\n\t"
  "v_exp_f32 v128, v104\n\t"
  "v_exp_f32 v129, v105\n\t"
  "v_cvt_pk_bf16_f32 v135, v130, v131\n\t"
  "v_exp_f32 v130, v106\n\t"
  "v_exp_f32 v131, v107\n\t"
  "v_cvt_pk_bf16_f32 v136, v128, v129\n\t"
  "v_mfma_f32_32x32x16_bf16 v[204:219], v[58:61], v[132:135], v[204:219]\n\t"
  "v_exp_f32 v128, v108\n\t"
  "v_exp_f32 v129, v109\n\t"
  "v_cvt_pk_bf16_f32 v137, v130, v131\n\t"
  "v_exp_f32 v130, v110\n\t"
  "v_exp_f32 v131, v111\n\t"
  "v_cvt_pk_bf16_f32 v138, v128, v129\n\t"
  "v_mfma_f32_32x32x16_bf16 v[96:111], v[48:51], v[80:83], 0\n\t"
  "buffer_load_dwordx4 v[196:199], v39, s[40:43], s54 offen offset:2048 sc1\n\t"
  "v_exp_f32 v128, v112\n\t"
  "v_exp_f32 v129, v113\n\t"
  "v_cvt_pk_bf16_f32 v139, v130, v131\n\t"
  "v_exp_f32 v130, v114\n\t"
  "v_exp_f32 v131, v115\n\t"
  "v_cvt_pk_bf16_f32 v132, v128, v129\n\t"
  "v_mfma_f32_32x32x16_bf16 v[204:219], v[252:255], v[136:139], v[204:219]\n\t"
  "v_exp_f32 v128, v116\n\t"
  "v_exp_f32 v129, v117\n\t"
  "v_cvt_pk_bf16_f32 v133, v130, v131\n\t"
  "v_exp_f32 v130, v118\n\t"
  "v_exp_f32 v131, v119\n\t"
  "v_cvt_pk_bf16_f32 v134, v128, v129\n\t"
  "v_mfma_f32_32x32x16_bf16 v[96:111], v[32:35], v[84:87], v[96:111]\n\t"
  "buffer_load_dwordx4 v[200:203], v39, s[40:43], s54 offen offset:3072 sc1\n\t"
  "v_exp_f32 v128, v120\n\t"
  "v_exp_f32 v129, v121\n\t"
  "v_cvt_pk_bf16_f32 v135, v130, v131\n\t"
  "v_exp_f32 v130, v122\n\t"
  "v_exp_f32 v131, v123\n\t"
  "v_cvt_pk_bf16_f32 v136, v128, v129\n\t"
  "v_mfma_f32_32x32x16_bf16 v[220:235], v[58:61], v[132:135], v[220:235]\n\t"
  "s_add_u32 s54, s54, 4096\n\t"
  "v_exp_f32 v128, v124\n\t"
  "v_exp_f32 v129, v125\n\t"
  "v_cvt_pk_bf16_f32 v137, v130, v131\n\t"
  "v_exp_f32 v130, v126\n\t"
  "v_exp_f32 v131, v127\n\t"
  "v_cvt_pk_bf16_f32 v138, v128, v129\n\t"
  "v_mfma_f32_32x32x16_bf16 v[112:127], v[48:51], v[88:91], 0\n\t"
  "s_and_b32 s54, s54, 0xffff\n\t"
  "v_exp_f32 v128, v96\n\t"
  "v_exp_f32 v129, v97\n\t"
  "v_cvt_pk_bf16_f32 v139, v130, v131\n\t"
  "v_exp_f32 v130, v98\n\t"
  "v_exp_f32 v131, v99\n\t"
  "v_cvt_pk_bf16_f32 v132, v128, v129\n\t"
  "v_mfma_f32_32x32x16_bf16 v[220:235], v[252:255], v[136:139], v[220:235]\n\t"
  "v_exp_f32 v128, v100\n\t"
  "v_exp_f32 v129, v101\n\t"
  "v_cvt_pk_bf16_f32 v133, v130, v131\n\t"
  "v_exp_f32 v130, v102\n\t"
  "v_exp_f32 v131, v103\n\t"
  "v_cvt_pk_bf16_f32 v134, v128, v129\n\t"
  "v_mfma_f32_32x32x16_bf16 v[112:127], v[32:35], v[92:95], v[112:127]\n\t"
  "v_exp_f32 v128, v104\n\t"
  "v_exp_f32 v129, v105\n\t"
  "v_cvt_pk_bf16_f32 v135, v130, v131\n\t"
  "v_exp_f32 v130, v106\n\t"
  "v_exp_f32 v131, v107\n\t"
  "v_cvt_pk_bf16_f32 v136, v128, v129\n\t"
  "v_mfma_f32_32x32x16_bf16 v[236:251], v[58:61], v[132:135], v[236:251]\n\t"
  "s_waitcnt vmcnt(12)\n\t"
  "v_exp_f32 v128, v108\n\t"
  "v_exp_f32 v129, v109\n\t"
  "v_cvt_pk_bf16_f32 v137, v130, v131\n\t"
  "v_exp_f32 v130, v110\n\t"
  "v_exp_f32 v131, v111\n\t"
  "v_cvt_pk_bf16_f32 v138, v128, v129\n\t"
  "v_mfma_f32_32x32x16_bf16 v[96:111], v[140:143], v[64:67], 0\n\t"
  "v_exp_f32 v128, v112\n\t"
  "v_exp_f32 v129, v113\n\t"
  "v_cvt_pk_bf16_f32 v139, v130, v131\n\t"
  "v_exp_f32 v130, v114\n\t"
  "v_exp_f32 v131, v115\n\t"
  "v_cvt_pk_bf16_f32 v132, v128, v129\n\t"
  "v_mfma_f32_32x32x16_bf16 v[236:251], v[252:255], v[136:139], v[236:251]\n\t"
  "v_exp_f32 v128, v116\n\t"
  "v_exp_f32 v129, v117\n\t"
  "v_cvt_pk_bf16_f32 v133, v130, v131\n\t"
  "v_exp_f32 v130, v118\n\t"
  "v_exp_f32 v131, v119\n\t"
  "v_cvt_pk_bf16_f32 v134, v128, v129\n\t"
  "v_mfma_f32_32x32x16_bf16 v[96:111], v[144:147], v[68:71], v[96:111]\n\t"
  "v_exp_f32 v128, v120\n\t"
  "v_exp_f32 v129, v121\n\t"
  "v_cvt_pk_bf16_f32 v135, v130, v131\n\t"
  "v_exp_f32 v130, v122\n\t"
  "v_exp_f32 v131, v123\n\t"
  "v_cvt_pk_bf16_f32 v136, v128, v129\n\t"
  "v_mfma_f32_32x32x16_bf16 v[16:31], v[58:61], v[132:135], v[16:31]\n\t"
  "v_exp_f32 v128, v124\n\t"
  "v_exp_f32 v129, v125\n\t"
  "v_cvt_pk_bf16_f32 v137, v130, v131\n\t"
  "v_exp_f32 v130, v126\n\t"
  "v_exp_f32 v131, v127\n\t"
  "v_cvt_pk_bf16_f32 v138, v128, v129\n\t"
  "v_mfma_f32_32x32x16_bf16 v[112:127], v[140:143], v[72:75], 0\n\t"
  "buffer_load_dwordx4 v[48:51], v39, s[40:43], s54 offen offset:0 sc1\n\t"
  "v_exp_f32 v128, v96\n\t"
  "v_exp_f32 v129, v97\n\t"
  "v_cvt_pk_bf16_f32 v139, v130, v131\n\t"
  "v_exp_f32 v130, v98\n\t"
  "v_exp_f32 v131, v99\n\t"
  "v_cvt_pk_bf16_f32 v132, v128, v129\n\t"
  "v_mfma_f32_32x32x16_bf16 v[16:31], v[252:255], v[136:139], v[16:31]\n\t"
  "v_exp_f32 v128, v100\n\t"
  "v_exp_f32 v129, v101\n\t"
  "v_cvt_pk_bf16_f32 v133, v130, v131\n\t"
  "v_exp_f32 v130, v102\n\t"
  "v_exp_f32 v131, v103\n\t"
  "v_cvt_pk_bf16_f32 v134, v128, v129\n\t"
  "v_mfma_f32_32x32x16_bf16 v[112:127], v[144:147], v[76:79], v[112:127]\n\t"
  "buffer_load_dwordx4 v[32:35], v39, s[40:43], s54 offen offset:1024 sc1\n\t"
  "v_exp_f32 v128, v104\n\t"
  "v_exp_f32 v129, v105\n\t"
  "v_cvt_pk_bf16_f32 v135, v130, v131\n\t"
  "v_exp_f32 v130, v106\n\t"
  "v_exp_f32 v131, v107\n\t"
  "v_cvt_pk_bf16_f32 v136, v128, v129\n\t"
  "v_mfma_f32_32x32x16_bf16 v[204:219], v[148:151], v[132:135], v[204:219]\n\t"
  "v_exp_f32 v128, v108\n\t"
  "v_exp_f32 v129, v109\n\t"
  "v_cvt_pk_bf16_f32 v137, v130, v131\n\t"
  "v_exp_f32 v130, v110\n\t"
  "v_exp_f32 v131, v111\n\t"
  "v_cvt_pk_bf16_f32 v138, v128, v129\n\t"
  "v_mfma_f32_32x32x16_bf16 v[96:111], v[140:143], v[80:83], 0\n\t"
  "buffer_load_dwordx4 v[58:61], v39, s[40:43], s54 offen offset:2048 sc1\n\t"
  "v_exp_f32 v128, v112\n\t"
  "v_exp_f32 v129, v113\n\t"
  "v_cvt_pk_bf16_f32 v139, v130, v131\n\t"
  "v_exp_f32 v130, v114\n\t"
  "v_exp_f32 v131, v115\n\t"
  "v_cvt_pk_bf16_f32 v132, v128, v129\n\t"
  "v_mfma_f32_32x32x16_bf16 v[204:219], v[152:155], v[136:139], v[204:219]\n\t"
  "v_exp_f32 v128, v116\n\t"
  "v_exp_f32 v129, v117\n\t"
  "v_cvt_pk_bf16_f32 v133, v130, v131\n\t"
  "v_exp_f32 v130, v118\n\t"
  "v_exp_f32 v131, v119\n\t"
  "v_cvt_pk_bf16_f32 v134, v128, v129\n\t"
  "v_mfma_f32_32x32x16_bf16 v[96:111], v[144:147], v[84:87], v[96:111]\n\t"
  "buffer_load_dwordx4 v[252:255], v39, s[40:43], s54 offen offset:3072 sc1\n\t"
  "v_exp_f32 v128, v120\n\t"
  "v_exp_f32 v129, v121\n\t"
  "v_cvt_pk_bf16_f32 v135, v130, v131\n\t"
  "v_exp_f32 v130, v122\n\t"
  "v_exp_f32 v131, v123\n\t"
  "v_cvt_pk_bf16_f32 v136, v128, v129\n\t"
  "v_mfma_f32_32x32x16_bf16 v[220:235], v[148:151], v[132:135], v[220:235]\n\t"
  "s_add_u32 s54, s54, 4096\n\t"
  "v_exp_f32 v128, v124\n\t"
  "v_exp_f32 v129, v125\n\t"
  "v_cvt_pk_bf16_f32 v137, v130, v131\n\t"
  "v_exp_f32 v130, v126\n\t"
  "v_exp_f32 v131, v127\n\t"
  "v_cvt_pk_bf16_f32 v138, v128, v129\n\t"
  "v_mfma_f32_32x32x16_bf16 v[112:127], v[140:143], v[88:91], 0\n\t"
  "s_and_b32 s54, s54, 0xffff\n\t"
  "v_exp_f32 v128, v96\n\t"
  "v_exp_f32 v129, v97\n\t"
  "v_cvt_pk_bf16_f32 v139, v130, v131\n\t"
  "v_exp_f32 v130, v98\n\t"
  "v_exp_f32 v131, v99\n\t"
  "v_cvt_pk_bf16_f32 v132, v128, v129\n\t"
  "v_mfma_f32_32x32x16_bf16 v[220:235], v[152:155], v[136:139], v[220:235]\n\t"
  "v_exp_f32 v128, v100\n\t"
  "v_exp_f32 v129, v101\n\t"
  "v_cvt_pk_bf16_f32 v133, v130, v131\n\t"
  "v_exp_f32 v130, v102\n\t"
  "v_exp_f32 v131, v103\n\t"
  "v_cvt_pk_bf16_f32 v134, v128, v129\n\t"
  "v_mfma_f32_32x32x16_bf16 v[112:127], v[144:147], v[92:95], v[112:127]\n\t"
  "v_exp_f32 v128, v104\n\t"
  "v_exp_f32 v129, v105\n\t"
  "v_cvt_pk_bf16_f32 v135, v130, v131\n\t"
  "v_exp_f32 v130, v106\n\t"
  "v_exp_f32 v131, v107\n\t"
  "v_cvt_pk_bf16_f32 v136, v128, v129\n\t"
  "v_mfma_f32_32x32x16_bf16 v[236:251], v[148:151], v[132:135], v[236:251]\n\t"
  "s_waitcnt vmcnt(12)\n\t"
  "v_exp_f32 v128, v108\n\t"
  "v_exp_f32 v129, v109\n\t"
  "v_cvt_pk_bf16_f32 v137, v130, v131\n\t"
  "v_exp_f32 v130, v110\n\t"
  "v_exp_f32 v131, v111\n\t"
  "v_cvt_pk_bf16_f32 v138, v128, v129\n\t"
  "v_mfma_f32_32x32x16_bf16 v[96:111], v[156:159], v[64:67], 0\n\t"
  "v_exp_f32 v128, v112\n\t"
  "v_exp_f32 v129, v113\n\t"
  "v_cvt_pk_bf16_f32 v139, v130, v131\n\t"
  "v_exp_f32 v130, v114\n\t"
  "v_exp_f32 v131, v115\n\t"
  "v_cvt_pk_bf16_f32 v132, v128, v129\n\t"
  "v_mfma_f32_32x32x16_bf16 v[236:251], v[152:155], v[136:139], v[236:251]\n\t"
  "v_exp_f32 v128, v116\n\t"
  "v_exp_f32 v129, v117\n\t"
  "v_cvt_pk_bf16_f32 v133, v130, v131\n\t"
  "v_exp_f32 v130, v118\n\t"
  "v_exp_f32 v131, v119\n\t"
  "v_cvt_pk_bf16_f32 v134, v128, v129\n\t"
  "v_mfma_f32_32x32x16_bf16 v[96:111], v[160:163], v[68:71], v[96:111]\n\t"
  "s_sub_u32 s55, s55, 1\n\t"
  "v_exp_f32 v128, v120\n\t"
  "v_exp_f32 v129, v121\n\t"
  "v_cvt_pk_bf16_f32 v135, v130, v131\n\t"
  "v_exp_f32 v130, v122\n\t"
  "v_exp_f32 v131, v123\n\t"
  "v_cvt_pk_bf16_f32 v136, v128, v129\n\t"
  "v_mfma_f32_32x32x16_bf16 v[16:31], v[148:151], v[132:135], v[16:31]\n\t"
  "s_cmp_lg_u32 s55, 0\n\t"
  "v_exp_f32 v128, v124\n\t"
  "v_exp_f32 v129, v125\n\t"
  "v_cvt_pk_bf16_f32 v137, v130, v131\n\t"
  "v_exp_f32 v130, v126\n\t"
  "v_exp_f32 v131, v127\n\t"
  "v_cvt_pk_bf16_f32 v138, v128, v129\n\t"
  "s_cbranch_scc1 LATT_%=\n\t"
  "v_mfma_f32_32x32x16_bf16 v[112:127], v[156:159], v[72:75], 0\n\t"
  "buffer_load_dwordx4 v[140:143], v39, s[40:43], s54 offen offset:0 sc1\n\t"
  "v_exp_f32 v128, v96\n\t"
  "v_exp_f32 v129, v97\n\t"
  "v_cvt_pk_bf16_f32 v139, v130, v131\n\t"
  "v_exp_f32 v130, v98\n\t"
  "v_exp_f32 v131, v99\n\t"
  "v_cvt_pk_bf16_f32 v132, v128, v129\n\t"
  "v_mfma_f32_32x32x16_bf16 v[16:31], v[152:155], v[136:139], v[16:31]\n\t"
  "v_exp_f32 v128, v100\n\t"
  "v_exp_f32 v129, v101\n\t"
  "v_cvt_pk_bf16_f32 v133, v130, v131\n\t"
  "v_exp_f32 v130, v102\n\t"
  "v_exp_f32 v131, v103\n\t"
  "v_cvt_pk_bf16_f32 v134, v128, v129\n\t"
  "v_mfma_f32_32x32x16_bf16 v[112:127], v[160:163], v[76:79], v[112:127]\n\t"
  "buffer_load_dwordx4 v[144:147], v39, s[40:43], s54 offen offset:1024 sc1\n\t"
  "v_exp_f32 v128, v104\n\t"
  "v_exp_f32 v129, v105\n\t"
  "v_cvt_pk_bf16_f32 v135, v130, v131\n\t"
  "v_exp_f32 v130, v106\n\t"
  "v_exp_f32 v131, v107\n\t"
  "v_cvt_pk_bf16_f32 v136, v128, v129\n\t"
  "v_mfma_f32_32x32x16_bf16 v[204:219], v[164:167], v[132:135], v[204:219]\n\t"
  "v_exp_f32 v128, v108\n\t"
  "v_exp_f32 v129, v109\n\t"
  "v_cvt_pk_bf16_f32 v137, v130, v131\n\t"
  "v_exp_f32 v130, v110\n\t"
  "v_exp_f32 v131, v111\n\t"
  "v_cvt_pk_bf16_f32 v138, v128, v129\n\t"
  "v_mfma_f32_32x32x16_bf16 v[96:111], v[156:159], v[80:83], 0\n\t"
  "buffer_load_dwordx4 v[148:151], v39, s[40:43], s54 offen offset:2048 sc1\n\t"
  "v_exp_f32 v128, v112\n\t"
  "v_exp_f32 v129, v113\n\t"
  "v_cvt_pk_bf16_f32 v139, v130, v131\n\t"
  "v_exp_f32 v130, v114\n\t"
  "v_exp_f32 v131, v115\n\t"
  "v_cvt_pk_bf16_f32 v132, v128, v129\n\t"
  "v_mfma_f32_32x32x16_bf16 v[204:219], v[168:171], v[136:139], v[204:219]\n\t"
  "v_exp_f32 v128, v116\n\t"
  "v_exp_f32 v129, v117\n\t"
  "v_cvt_pk_bf16_f32 v133, v130, v131\n\t"
  "v_exp_f32 v130, v118\n\t"
  "v_exp_f32 v131, v119\n\t"
  "v_cvt_pk_bf16_f32 v134, v128, v129\n\t"
  "v_mfma_f32_32x32x16_bf16 v[96:111], v[160:163], v[84:87], v[96:111]\n\t"
  "buffer_load_dwordx4 v[152:155], v39, s[40:43], s54 offen offset:3072 sc1\n\t"
  "v_exp_f32 v128, v120\n\t"
  "v_exp_f32 v129, v121\n\t"
  "v_cvt_pk_bf16_f32 v135, v130, v131\n\t"
  "v_exp_f32 v130, v122\n\t"
  "v_exp_f32 v131, v123\n\t"
  "v_cvt_pk_bf16_f32 v136, v128, v129\n\t"
  "v_mfma_f32_32x32x16_bf16 v[220:235], v[164:167], v[132:135], v[220:235]\n\t"
  "s_add_u32 s54, s54, 4096\n\t"
  "v_exp_f32 v128, v124\n\t"
  "v_exp_f32 v129, v125\n\t"
  "v_cvt_pk_bf16_f32 v137, v130, v131\n\t"
  "v_exp_f32 v130, v126\n\t"
  "v_exp_f32 v131, v127\n\t"
  "v_cvt_pk_bf16_f32 v138, v128, v129\n\t"
  "v_mfma_f32_32x32x16_bf16 v[112:127], v[156:159], v[88:91], 0\n\t"
  "s_and_b32 s54, s54, 0xffff\n\t"
  "v_exp_f32 v128, v96\n\t"
  "v_exp_f32 v129, v97\n\t"
  "v_cvt_pk_bf16_f32 v139, v130, v131\n\t"
  "v_exp_f32 v130, v98\n\t"
  "v_exp_f32 v131, v99\n\t"
  "v_cvt_pk_bf16_f32 v132, v128, v129\n\t"
  "v_mfma_f32_32x32x16_bf16 v[220:235], v[168:171], v[136:139], v[220:235]\n\t"
  "v_exp_f32 v128, v100\n\t"
  "v_exp_f32 v129, v101\n\t"
  "v_cvt_pk_bf16_f32 v133, v130, v131\n\t"
  "v_exp_f32 v130, v102\n\t"
  "v_exp_f32 v131, v103\n\t"
  "v_cvt_pk_bf16_f32 v134, v128, v129\n\t"
  "v_mfma_f32_32x32x16_bf16 v[112:127], v[160:163], v[92:95], v[112:127]\n\t"
  "v_exp_f32 v128, v104\n\t"
  "v_exp_f32 v129, v105\n\t"
  "v_cvt_pk_bf16_f32 v135, v130, v131\n\t"
  "v_exp_f32 v130, v106\n\t"
  "v_exp_f32 v131, v107\n\t"
  "v_cvt_pk_bf16_f32 v136, v128, v129\n\t"
  "v_mfma_f32_32x32x16_bf16 v[236:251], v[164:167], v[132:135], v[236:251]\n\t"
  "s_waitcnt vmcnt(12)\n\t"
  "v_exp_f32 v128, v108\n\t"
  "v_exp_f32 v129, v109\n\t"
  "v_cvt_pk_bf16_f32 v137, v130, v131\n\t"
  "v_exp_f32 v130, v110\n\t"
  "v_exp_f32 v131, v111\n\t"
  "v_cvt_pk_bf16_f32 v138, v128, v129\n\t"
  "v_mfma_f32_32x32x16_bf16 v[96:111], v[172:175], v[64:67], 0\n\t"
  "v_exp_f32 v128, v112\n\t"
  "v_exp_f32 v129, v113\n\t"
  "v_cvt_pk_bf16_f32 v139, v130, v131\n\t"
  "v_exp_f32 v130, v114\n\t"
  "v_exp_f32 v131, v115\n\t"
  "v_cvt_pk_bf16_f32 v132, v128, v129\n\t"
  "v_mfma_f32_32x32x16_bf16 v[236:251], v[168:171], v[136:139], v[236:251]\n\t"
  "v_exp_f32 v128, v116\n\t"
  "v_exp_f32 v129, v117\n\t"
  "v_cvt_pk_bf16_f32 v133, v130, v131\n\t"
  "v_exp_f32 v130, v118\n\t"
  "v_exp_f32 v131, v119\n\t"
  "v_cvt_pk_bf16_f32 v134, v128, v129\n\t"
  "v_mfma_f32_32x32x16_bf16 v[96:111], v[176:179], v[68:71], v[96:111]\n\t"
  "v_exp_f32 v128, v120\n\t"
  "v_exp_f32 v129, v121\n\t"
  "v_cvt_pk_bf16_f32 v135, v130, v131\n\t"
  "v_exp_f32 v130, v122\n\t"
  "v_exp_f32 v131, v123\n\t"
  "v_cvt_pk_bf16_f32 v136, v128, v129\n\t"
  "v_mfma_f32_32x32x16_bf16 v[16:31], v[164:167], v[132:135], v[16:31]\n\t"
  "v_exp_f32 v128, v124\n\t"
  "v_exp_f32 v129, v125\n\t"
  "v_cvt_pk_bf16_f32 v137, v130, v131\n\t"
  "v_exp_f32 v130, v126\n\t"
  "v_exp_f32 v131, v127\n\t"
  "v_cvt_pk_bf16_f32 v138, v128, v129\n\t"
  "v_mfma_f32_32x32x16_bf16 v[112:127], v[172:175], v[72:75], 0\n\t"
  "v_exp_f32 v128, v96\n\t"
  "v_exp_f32 v129, v97\n\t"
  "v_cvt_pk_bf16_f32 v139, v130, v131\n\t"
  "v_exp_f32 v130, v98\n\t"
  "v_exp_f32 v131, v99\n\t"
  "v_cvt_pk_bf16_f32 v132, v128, v129\n\t"
  "v_mfma_f32_32x32x16_bf16 v[16:31], v[168:171], v[136:139], v[16:31]\n\t"
  "v_exp_f32 v128, v100\n\t"
  "v_exp_f32 v129, v101\n\t"
  "v_cvt_pk_bf16_f32 v133, v130, v131\n\t"
  "v_exp_f32 v130, v102\n\t"
  "v_exp_f32 v131, v103\n\t"
  "v_cvt_pk_bf16_f32 v134, v128, v129\n\t"
  "v_mfma_f32_32x32x16_bf16 v[112:127], v[176:179], v[76:79], v[112:127]\n\t"
  "v_exp_f32 v128, v104\n\t"
  "v_exp_f32 v129, v105\n\t"
  "v_cvt_pk_bf16_f32 v135, v130, v131\n\t"
  "v_exp_f32 v130, v106\n\t"
  "v_exp_f32 v131, v107\n\t"
  "v_cvt_pk_bf16_f32 v136, v128, v129\n\t"
  "v_mfma_f32_32x32x16_bf16 v[204:219], v[180:183], v[132:135], v[204:219]\n\t"
  "v_exp_f32 v128, v108\n\t"
  "v_exp_f32 v129, v109\n\t"
  "v_cvt_pk_bf16_f32 v137, v130, v131\n\t"
  "v_exp_f32 v130, v110\n\t"
  "v_exp_f32 v131, v111\n\t"
  "v_cvt_pk_bf16_f32 v138, v128, v129\n\t"
  "v_mfma_f32_32x32x16_bf16 v[96:111], v[172:175], v[80:83], 0\n\t"
  "v_exp_f32 v128, v112\n\t"
  "v_exp_f32 v129, v113\n\t"
  "v_cvt_pk_bf16_f32 v139, v130, v131\n\t"
  "v_exp_f32 v130, v114\n\t"
  "v_exp_f32 v131, v115\n\t"
  "v_cvt_pk_bf16_f32 v132, v128, v129\n\t"
  "v_mfma_f32_32x32x16_bf16 v[204:219], v[184:187], v[136:139], v[204:219]\n\t"
  "v_exp_f32 v128, v116\n\t"
  "v_exp_f32 v129, v117\n\t"
  "v_cvt_pk_bf16_f32 v133, v130, v131\n\t"
  "v_exp_f32 v130, v118\n\t"
  "v_exp_f32 v131, v119\n\t"
  "v_cvt_pk_bf16_f32 v134, v128, v129\n\t"
  "v_mfma_f32_32x32x16_bf16 v[96:111], v[176:179], v[84:87], v[96:111]\n\t"
  "v_exp_f32 v128, v120\n\t"
  "v_exp_f32 v129, v121\n\t"
  "v_cvt_pk_bf16_f32 v135, v130, v131\n\t"
  "v_exp_f32 v130, v122\n\t"
  "v_exp_f32 v131, v123\n\t"
  "v_cvt_pk_bf16_f32 v136, v128, v129\n\t"
  "v_mfma_f32_32x32x16_bf16 v[220:235], v[180:183], v[132:135], v[220:235]\n\t"
  "v_exp_f32 v128, v124\n\t"
  "v_exp_f32 v129, v125\n\t"
  "v_cvt_pk_bf16_f32 v137, v130, v131\n\t"
  "v_exp_f32 v130, v126\n\t"
  "v_exp_f32 v131, v127\n\t"
  "v_cvt_pk_bf16_f32 v138, v128, v129\n\t"
  "v_mfma_f32_32x32x16_bf16 v[112:127], v[172:175], v[88:91], 0\n\t"
  "v_exp_f32 v128, v96\n\t"
  "v_exp_f32 v129, v97\n\t"
  "v_cvt_pk_bf16_f32 v139, v130, v131\n\t"
  "v_exp_f32 v130, v98\n\t"
  "v_exp_f32 v131, v99\n\t"
  "v_cvt_pk_bf16_f32 v132, v128, v129\n\t"
  "v_mfma_f32_32x32x16_bf16 v[220:235], v[184:187], v[136:139], v[220:235]\n\t"
  "v_exp_f32 v128, v100\n\t"
  "v_exp_f32 v129, v101\n\t"
  "v_cvt_pk_bf16_f32 v133, v130, v131\n\t"
  "v_exp_f32 v130, v102\n\t"
  "v_exp_f32 v131, v103\n\t"
  "v_cvt_pk_bf16_f32 v134, v128, v129\n\t"
  "v_mfma_f32_32x32x16_bf16 v[112:127], v[176:179], v[92:95], v[112:127]\n\t"
  "v_exp_f32 v128, v104\n\t"
  "v_exp_f32 v129, v105\n\t"
  "v_cvt_pk_bf16_f32 v135, v130, v131\n\t"
  "v_exp_f32 v130, v106\n\t"
  "v_exp_f32 v131, v107\n\t"
  "v_cvt_pk_bf16_f32 v136, v128, v129\n\t"
  "v_mfma_f32_32x32x16_bf16 v[236:251], v[180:183], v[132:135], v[236:251]\n\t"
  "s_waitcnt vmcnt(8)\n\t"
  "v_exp_f32 v128, v108\n\t"
  "v_exp_f32 v129, v109\n\t"
  "v_cvt_pk_bf16_f32 v137, v130, v131\n\t"
  "v_exp_f32 v130, v110\n\t"
  "v_exp_f32 v131, v111\n\t"
  "v_cvt_pk_bf16_f32 v138, v128, v129\n\t"
  "v_mfma_f32_32x32x16_bf16 v[96:111], v[188:191], v[64:67], 0\n\t"
  "v_exp_f32 v128, v112\n\t"
  "v_exp_f32 v129, v113\n\t"
  "v_cvt_pk_bf16_f32 v139, v130, v131\n\t"
  "v_exp_f32 v130, v114\n\t"
  "v_exp_f32 v131, v115\n\t"
  "v_cvt_pk_bf16_f32 v132, v128, v129\n\t"
  "v_mfma_f32_32x32x16_bf16 v[236:251], v[184:187], v[136:139], v[236:251]\n\t"
  "v_exp_f32 v128, v116\n\t"
  "v_exp_f32 v129, v117\n\t"
  "v_cvt_pk_bf16_f32 v133, v130, v131\n\t"
  "v_exp_f32 v130, v118\n\t"
  "v_exp_f32 v131, v119\n\t"
  "v_cvt_pk_bf16_f32 v134, v128, v129\n\t"
  "v_mfma_f32_32x32x16_bf16 v[96:111], v[192:195], v[68:71], v[96:111]\n\t"
  "v_exp_f32 v128, v120\n\t"
  "v_exp_f32 v129, v121\n\t"
  "v_cvt_pk_bf16_f32 v135, v130, v131\n\t"
  "v_exp_f32 v130, v122\n\t"
  "v_exp_f32 v131, v123\n\t"
  "v_cvt_pk_bf16_f32 v136, v128, v129\n\t"
  "v_mfma_f32_32x32x16_bf16 v[16:31], v[180:183], v[132:135], v[16:31]\n\t"
  "v_exp_f32 v128, v124\n\t"
  "v_exp_f32 v129, v125\n\t"
  "v_cvt_pk_bf16_f32 v137, v130, v131\n\t"
  "v_exp_f32 v130, v126\n\t"
  "v_exp_f32 v131, v127\n\t"
  "v_cvt_pk_bf16_f32 v138, v128, v129\n\t"
  "v_mfma_f32_32x32x16_bf16 v[112:127], v[188:191], v[72:75], 0\n\t"
  "buffer_load_dwordx2 v[12:13], v46, s[36:39], 0 offen sc1\n\t"
  "v_exp_f32 v128, v96\n\t"
  "v_exp_f32 v129, v97\n\t"
  "v_cvt_pk_bf16_f32 v139, v130, v131\n\t"
  "v_exp_f32 v130, v98\n\t"
  "v_exp_f32 v131, v99\n\t"
  "v_cvt_pk_bf16_f32 v132, v128, v129\n\t"
  "v_mfma_f32_32x32x16_bf16 v[16:31], v[184:187], v[136:139], v[16:31]\n\t"
  "v_exp_f32 v128, v100\n\t"
  "v_exp_f32 v129, v101\n\t"
  "v_cvt_pk_bf16_f32 v133, v130, v131\n\t"
  "v_exp_f32 v130, v102\n\t"
  "v_exp_f32 v131, v103\n\t"
  "v_cvt_pk_bf16_f32 v134, v128, v129\n\t"
  "v_mfma_f32_32x32x16_bf16 v[112:127], v[192:195], v[76:79], v[112:127]\n\t"
  "v_exp_f32 v128, v104\n\t"
  "v_exp_f32 v129, v105\n\t"
  "v_cvt_pk_bf16_f32 v135, v130, v131\n\t"
  "v_exp_f32 v130, v106\n\t"
  "v_exp_f32 v131, v107\n\t"
  "v_cvt_pk_bf16_f32 v136, v128, v129\n\t"
  "v_mfma_f32_32x32x16_bf16 v[204:219], v[196:199], v[132:135], v[204:219]\n\t"
  "v_exp_f32 v128, v108\n\t"
  "v_exp_f32 v129, v109\n\t"
  "v_cvt_pk_bf16_f32 v137, v130, v131\n\t"
  "v_exp_f32 v130, v110\n\t"
  "v_exp_f32 v131, v111\n\t"
  "v_cvt_pk_bf16_f32 v138, v128, v129\n\t"
  "v_mfma_f32_32x32x16_bf16 v[96:111], v[188:191], v[80:83], 0\n\t"
  "v_exp_f32 v128, v112\n\t"
  "v_exp_f32 v129, v113\n\t"
  "v_cvt_pk_bf16_f32 v139, v130, v131\n\t"
  "v_exp_f32 v130, v114\n\t"
  "v_exp_f32 v131, v115\n\t"
  "v_cvt_pk_bf16_f32 v132, v128, v129\n\t"
  "v_mfma_f32_32x32x16_bf16 v[204:219], v[200:203], v[136:139], v[204:219]\n\t"
  "v_exp_f32 v128, v116\n\t"
  "v_exp_f32 v129, v117\n\t"
  "v_cvt_pk_bf16_f32 v133, v130, v131\n\t"
  "v_exp_f32 v130, v118\n\t"
  "v_exp_f32 v131, v119\n\t"
  "v_cvt_pk_bf16_f32 v134, v128, v129\n\t"
  "v_mfma_f32_32x32x16_bf16 v[96:111], v[192:195], v[84:87], v[96:111]\n\t"
  "v_exp_f32 v128, v120\n\t"
  "v_exp_f32 v129, v121\n\t"
  "v_cvt_pk_bf16_f32 v135, v130, v131\n\t"
  "v_exp_f32 v130, v122\n\t"
  "v_exp_f32 v131, v123\n\t"
  "v_cvt_pk_bf16_f32 v136, v128, v129\n\t"
  "v_mfma_f32_32x32x16_bf16 v[220:235], v[196:199], v[132:135], v[220:235]\n\t"
  "v_exp_f32 v128, v124\n\t"
  "v_exp_f32 v129, v125\n\t"
  "v_cvt_pk_bf16_f32 v137, v130, v131\n\t"
  "v_exp_f32 v130, v126\n\t"
  "v_exp_f32 v131, v127\n\t"
  "v_cvt_pk_bf16_f32 v138, v128, v129\n\t"
  "v_mfma_f32_32x32x16_bf16 v[112:127], v[188:191], v[88:91], 0\n\t"
  "v_exp_f32 v128, v96\n\t"
  "v_exp_f32 v129, v97\n\t"
  "v_cvt_pk_bf16_f32 v139, v130, v131\n\t"
  "v_exp_f32 v130, v98\n\t"
  "v_exp_f32 v131, v99\n\t"
  "v_cvt_pk_bf16_f32 v132, v128, v129\n\t"
  "v_mfma_f32_32x32x16_bf16 v[220:235], v[200:203], v[136:139], v[220:235]\n\t"
  "v_exp_f32 v128, v100\n\t"
  "v_exp_f32 v129, v101\n\t"
  "v_cvt_pk_bf16_f32 v133, v130, v131\n\t"
  "v_exp_f32 v130, v102\n\t"
  "v_exp_f32 v131, v103\n\t"
  "v_cvt_pk_bf16_f32 v134, v128, v129\n\t"
  "v_mfma_f32_32x32x16_bf16 v[112:127], v[192:195], v[92:95], v[112:127]\n\t"
  "v_exp_f32 v128, v104\n\t"
  "v_exp_f32 v129, v105\n\t"
  "v_cvt_pk_bf16_f32 v135, v130, v131\n\t"
  "v_exp_f32 v130, v106\n\t"
  "v_exp_f32 v131, v107\n\t"
  "v_cvt_pk_bf16_f32 v136, v128, v129\n\t"
  "v_mfma_f32_32x32x16_bf16 v[236:251], v[196:199], v[132:135], v[236:251]\n\t"
  "s_waitcnt vmcnt(5)\n\t"
  "v_exp_f32 v128, v108\n\t"
  "v_exp_f32 v129, v109\n\t"
  "v_cvt_pk_bf16_f32 v137, v130, v131\n\t"
  "v_exp_f32 v130, v110\n\t"
  "v_exp_f32 v131, v111\n\t"
  "v_cvt_pk_bf16_f32 v138, v128, v129\n\t"
  "v_mfma_f32_32x32x16_bf16 v[96:111], v[48:51], v[64:67], 0\n\t"
  "v_exp_f32 v128, v112\n\t"
  "v_exp_f32 v129, v113\n\t"
  "v_cvt_pk_bf16_f32 v139, v130, v131\n\t"
  "v_exp_f32 v130, v114\n\t"
  "v_exp_f32 v131, v115\n\t"
  "v_cvt_pk_bf16_f32 v132, v128, v129\n\t"
  "v_mfma_f32_32x32x16_bf16 v[236:251], v[200:203], v[136:139], v[236:251]\n\t"
  "v_exp_f32 v128, v116\n\t"
  "v_exp_f32 v129, v117\n\t"
  "v_cvt_pk_bf16_f32 v133, v130, v131\n\t"
  "v_exp_f32 v130, v118\n\t"
  "v_exp_f32 v131, v119\n\t"
  "v_cvt_pk_bf16_f32 v134, v128, v129\n\t"
  "v_mfma_f32_32x32x16_bf16 v[96:111], v[32:35], v[68:71], v[96:111]\n\t"
  "v_exp_f32 v128, v120\n\t"
  "v_exp_f32 v129, v121\n\t"
  "v_cvt_pk_bf16_f32 v135, v130, v131\n\t"
  "v_exp_f32 v130, v122\n\t"
  "v_exp_f32 v131, v123\n\t"
  "v_cvt_pk_bf16_f32 v136, v128, v129\n\t"
  "v_mfma_f32_32x32x16_bf16 v[16:31], v[196:199], v[132:135], v[16:31]\n\t"
  "v_exp_f32 v128, v124\n\t"
  "v_exp_f32 v129, v125\n\t"
  "v_cvt_pk_bf16_f32 v137, v130, v131\n\t"
  "v_exp_f32 v130, v126\n\t"
  "v_exp_f32 v131, v127\n\t"
  "v_cvt_pk_bf16_f32 v138, v128, v129\n\t"
  "v_mfma_f32_32x32x16_bf16 v[112:127], v[48:51], v[72:75], 0\n\t"
  "v_exp_f32 v128, v96\n\t"
  "v_exp_f32 v129, v97\n\t"
  "v_cvt_pk_bf16_f32 v139, v130, v131\n\t"
  "v_exp_f32 v130, v98\n\t"
  "v_exp_f32 v131, v99\n\t"
  "v_cvt_pk_bf16_f32 v132, v128, v129\n\t"
  "v_mfma_f32_32x32x16_bf16 v[16:31], v[200:203], v[136:139], v[16:31]\n\t"
  "v_exp_f32 v128, v100\n\t"
  "v_exp_f32 v129, v101\n\t"
  "v_cvt_pk_bf16_f32 v133, v130, v131\n\t"
  "v_exp_f32 v130, v102\n\t"
  "v_exp_f32 v131, v103\n\t"
  "v_cvt_pk_bf16_f32 v134, v128, v129\n\t"
  "v_mfma_f32_32x32x16_bf16 v[112:127], v[32:35], v[76:79], v[112:127]\n\t"
  "v_exp_f32 v128, v104\n\t"
  "v_exp_f32 v129, v105\n\t"
  "v_cvt_pk_bf16_f32 v135, v130, v131\n\t"
  "v_exp_f32 v130, v106\n\t"
  "v_exp_f32 v131, v107\n\t"
  "v_cvt_pk_bf16_f32 v136, v128, v129\n\t"
  "v_mfma_f32_32x32x16_bf16 v[204:219], v[58:61], v[132:135], v[204:219]\n\t"
  "v_exp_f32 v128, v108\n\t"
  "v_exp_f32 v129, v109\n\t"
  "v_cvt_pk_bf16_f32 v137, v130, v131\n\t"
  "v_exp_f32 v130, v110\n\t"
  "v_exp_f32 v131, v111\n\t"
  "v_cvt_pk_bf16_f32 v138, v128, v129\n\t"
  "v_mfma_f32_32x32x16_bf16 v[96:111], v[48:51], v[80:83], 0\n\t"
  "v_exp_f32 v128, v112\n\t"
  "v_exp_f32 v129, v113\n\t"
  "v_cvt_pk_bf16_f32 v139, v130, v131\n\t"
  "v_exp_f32 v130, v114\n\t"
  "v_exp_f32 v131, v115\n\t"
  "v_cvt_pk_bf16_f32 v132, v128, v129\n\t"
  "v_mfma_f32_32x32x16_bf16 v[204:219], v[252:255], v[136:139], v[204:219]\n\t"
  "v_exp_f32 v128, v116\n\t"
  "v_exp_f32 v129, v117\n\t"
  "v_cvt_pk_bf16_f32 v133, v130, v131\n\t"
  "v_exp_f32 v130, v118\n\t"
  "v_exp_f32 v131, v119\n\t"
  "v_cvt_pk_bf16_f32 v134, v128, v129\n\t"
  "v_mfma_f32_32x32x16_bf16 v[96:111], v[32:35], v[84:87], v[96:111]\n\t"
  "v_exp_f32 v128, v120\n\t"
  "v_exp_f32 v129, v121\n\t"
  "v_cvt_pk_bf16_f32 v135, v130, v131\n\t"
  "v_exp_f32 v130, v122\n\t"
  "v_exp_f32 v131, v123\n\t"
  "v_cvt_pk_bf16_f32 v136, v128, v129\n\t"
  "v_mfma_f32_32x32x16_bf16 v[220:235], v[58:61], v[132:135], v[220:235]\n\t"
  "v_exp_f32 v128, v124\n\t"
  "v_exp_f32 v129, v125\n\t"
  "v_cvt_pk_bf16_f32 v137, v130, v131\n\t"
  "v_exp_f32 v130, v126\n\t"
  "v_exp_f32 v131, v127\n\t"
  "v_cvt_pk_bf16_f32 v138, v128, v129\n\t"
  "v_mfma_f32_32x32x16_bf16 v[112:127], v[48:51], v[88:91], 0\n\t"
  "v_exp_f32 v128, v96\n\t"
  "v_exp_f32 v129, v97\n\t"
  "v_cvt_pk_bf16_f32 v139, v130, v131\n\t"
  "v_exp_f32 v130, v98\n\t"
  "v_exp_f32 v131, v99\n\t"
  "v_cvt_pk_bf16_f32 v132, v128, v129\n\t"
  "v_mfma_f32_32x32x16_bf16 v[220:235], v[252:255], v[136:139], v[220:235]\n\t"
  "v_exp_f32 v128, v100\n\t"
  "v_exp_f32 v129, v101\n\t"
  "v_cvt_pk_bf16_f32 v133, v130, v131\n\t"
  "v_exp_f32 v130, v102\n\t"
  "v_exp_f32 v131, v103\n\t"
  "v_cvt_pk_bf16_f32 v134, v128, v129\n\t"
  "v_mfma_f32_32x32x16_bf16 v[112:127], v[32:35], v[92:95], v[112:127]\n\t"
  "v_exp_f32 v128, v104\n\t"
  "v_exp_f32 v129, v105\n\t"
  "v_cvt_pk_bf16_f32 v135, v130, v131\n\t"
  "v_exp_f32 v130, v106\n\t"
  "v_exp_f32 v131, v107\n\t"
  "v_cvt_pk_bf16_f32 v136, v128, v129\n\t"
  "v_mfma_f32_32x32x16_bf16 v[236:251], v[58:61], v[132:135], v[236:251]\n\t"
  "s_waitcnt vmcnt(0)\n\t"
  "v_exp_f32 v128, v108\n\t"
  "v_exp_f32 v129, v109\n\t"
  "v_cvt_pk_bf16_f32 v137, v130, v131\n\t"
  "v_exp_f32 v130, v110\n\t"
  "v_exp_f32 v131, v111\n\t"
  "v_cvt_pk_bf16_f32 v138, v128, v129\n\t"
  "v_mfma_f32_32x32x16_bf16 v[96:111], v[140:143], v[64:67], 0\n\t"
  "v_exp_f32 v128, v112\n\t"
  "v_exp_f32 v129, v113\n\t"
  "v_cvt_pk_bf16_f32 v139, v130, v131\n\t"
  "v_exp_f32 v130, v114\n\t"
  "v_exp_f32 v131, v115\n\t"
  "v_cvt_pk_bf16_f32 v132, v128, v129\n\t"
  "v_mfma_f32_32x32x16_bf16 v[236:251], v[252:255], v[136:139], v[236:251]\n\t"
  "v_exp_f32 v128, v116\n\t"
  "v_exp_f32 v129, v117\n\t"
  "v_cvt_pk_bf16_f32 v133, v130, v131\n\t"
  "v_exp_f32 v130, v118\n\t"
  "v_exp_f32 v131, v119\n\t"
  "v_cvt_pk_bf16_f32 v134, v128, v129\n\t"
  "v_mfma_f32_32x32x16_bf16 v[96:111], v[144:147], v[68:71], v[96:111]\n\t"
  "v_exp_f32 v128, v120\n\t"
  "v_exp_f32 v129, v121\n\t"
  "v_cvt_pk_bf16_f32 v135, v130, v131\n\t"
  "v_exp_f32 v130, v122\n\t"
  "v_exp_f32 v131, v123\n\t"
  "v_cvt_pk_bf16_f32 v136, v128, v129\n\t"
  "v_mfma_f32_32x32x16_bf16 v[16:31], v[58:61], v[132:135], v[16:31]\n\t"
  "v_exp_f32 v128, v124\n\t"
  "v_exp_f32 v129, v125\n\t"
  "v_cvt_pk_bf16_f32 v137, v130, v131\n\t"
  "v_exp_f32 v130, v126\n\t"
  "v_exp_f32 v131, v127\n\t"
  "v_cvt_pk_bf16_f32 v138, v128, v129\n\t"
  "v_mfma_f32_32x32x16_bf16 v[112:127], v[140:143], v[72:75], 0\n\t"
  "v_exp_f32 v128, v96\n\t"
  "v_exp_f32 v129, v97\n\t"
  "v_cvt_pk_bf16_f32 v139, v130, v131\n\t"
  "v_exp_f32 v130, v98\n\t"
  "v_exp_f32 v131, v99\n\t"
  "v_cvt_pk_bf16_f32 v132, v128, v129\n\t"
  "v_mfma_f32_32x32x16_bf16 v[16:31], v[252:255], v[136:139], v[16:31]\n\t"
  "v_exp_f32 v128, v100\n\t"
  "v_exp_f32 v129, v101\n\t"
  "v_cvt_pk_bf16_f32 v133, v130, v131\n\t"
  "v_exp_f32 v130, v102\n\t"
  "v_exp_f32 v131, v103\n\t"
  "v_cvt_pk_bf16_f32 v134, v128, v129\n\t"
  "v_mfma_f32_32x32x16_bf16 v[112:127], v[144:147], v[76:79], v[112:127]\n\t"
  "v_exp_f32 v128, v104\n\t"
  "v_exp_f32 v129, v105\n\t"
  "v_cvt_pk_bf16_f32 v135, v130, v131\n\t"
  "v_exp_f32 v130, v106\n\t"
  "v_exp_f32 v131, v107\n\t"
  "v_cvt_pk_bf16_f32 v136, v128, v129\n\t"
  "v_mfma_f32_32x32x16_bf16 v[204:219], v[148:151], v[132:135], v[204:219]\n\t"
  "v_exp_f32 v128, v108\n\t"
  "v_exp_f32 v129, v109\n\t"
  "v_cvt_pk_bf16_f32 v137, v130, v131\n\t"
  "v_exp_f32 v130, v110\n\t"
  "v_exp_f32 v131, v111\n\t"
  "v_cvt_pk_bf16_f32 v138, v128, v129\n\t"
  "v_mfma_f32_32x32x16_bf16 v[96:111], v[140:143], v[80:83], 0\n\t"
  "v_exp_f32 v128, v112\n\t"
  "v_exp_f32 v129, v113\n\t"
  "v_cvt_pk_bf16_f32 v139, v130, v131\n\t"
  "v_exp_f32 v130, v114\n\t"
  "v_exp_f32 v131, v115\n\t"
  "v_cvt_pk_bf16_f32 v132, v128, v129\n\t"
  "v_mfma_f32_32x32x16_bf16 v[204:219], v[152:155], v[136:139], v[204:219]\n\t"
  "v_exp_f32 v128, v116\n\t"
  "v_exp_f32 v129, v117\n\t"
  "v_cvt_pk_bf16_f32 v133, v130, v131\n\t"
  "v_exp_f32 v130, v118\n\t"
  "v_exp_f32 v131, v119\n\t"
  "v_cvt_pk_bf16_f32 v134, v128, v129\n\t"
  "v_mfma_f32_32x32x16_bf16 v[96:111], v[144:147], v[84:87], v[96:111]\n\t"
  "v_exp_f32 v128, v120\n\t"
  "v_exp_f32 v129, v121\n\t"
  "v_cvt_pk_bf16_f32 v135, v130, v131\n\t"
  "v_exp_f32 v130, v122\n\t"
  "v_exp_f32 v131, v123\n\t"
  "v_cvt_pk_bf16_f32 v136, v128, v129\n\t"
  "v_mfma_f32_32x32x16_bf16 v[220:235], v[148:151], v[132:135], v[220:235]\n\t"
  "ds_write_b128 v42, v[204:207] offset:0\n\t"
  "v_exp_f32 v128, v124\n\t"
  "v_exp_f32 v129, v125\n\t"
  "v_cvt_pk_bf16_f32 v137, v130, v131\n\t"
  "v_exp_f32 v130, v126\n\t"
  "v_exp_f32 v131, v127\n\t"
  "v_cvt_pk_bf16_f32 v138, v128, v129\n\t"
  "v_mfma_f32_32x32x16_bf16 v[112:127], v[140:143], v[88:91], 0\n\t"
  "ds_write_b128 v42, v[208:211] offset:1024\n\t"
  "v_exp_f32 v128, v96\n\t"
  "v_exp_f32 v129, v97\n\t"
  "v_cvt_pk_bf16_f32 v139, v130, v131\n\t"
  "v_exp_f32 v130, v98\n\t"
  "v_exp_f32 v131, v99\n\t"
  "v_cvt_pk_bf16_f32 v132, v128, v129\n\t"
  "v_mfma_f32_32x32x16_bf16 v[220:235], v[152:155], v[136:139], v[220:235]\n\t"
  "ds_write_b128 v42, v[212:215] offset:2048\n\t"
  "v_exp_f32 v128, v100\n\t"
  "v_exp_f32 v129, v101\n\t"
  "v_cvt_pk_bf16_f32 v133, v130, v131\n\t"
  "v_exp_f32 v130, v102\n\t"
  "v_exp_f32 v131, v103\n\t"
  "v_cvt_pk_bf16_f32 v134, v128, v129\n\t"
  "v_mfma_f32_32x32x16_bf16 v[112:127], v[144:147], v[92:95], v[112:127]\n\t"
  "ds_write_b128 v42, v[216:219] offset:3072\n\t"
  "v_exp_f32 v128, v104\n\t"
  "v_exp_f32 v129, v105\n\t"
  "v_cvt_pk_bf16_f32 v135, v130, v131\n\t"
  "v_exp_f32 v130, v106\n\t"
  "v_exp_f32 v131, v107\n\t"
  "v_cvt_pk_bf16_f32 v136, v128, v129\n\t"
  "v_mfma_f32_32x32x16_bf16 v[236:251], v[148:151], v[132:135], v[236:251]\n\t"
  "ds_write_b128 v43, v[220:223] offset:0\n\t"
  "v_exp_f32 v128, v108\n\t"
  "v_exp_f32 v129, v109\n\t"
  "v_cvt_pk_bf16_f32 v137, v130, v131\n\t"
  "v_exp_f32 v130, v110\n\t"
  "v_exp_f32 v131, v111\n\t"
  "v_cvt_pk_bf16_f32 v138, v128, v129\n\t"
  "ds_write_b128 v43, v[224:227] offset:1024\n\t"
  "v_exp_f32 v128, v112\n\t"
  "v_exp_f32 v129, v113\n\t"
  "v_cvt_pk_bf16_f32 v139, v130, v131\n\t"
  "v_exp_f32 v130, v114\n\t"
  "v_exp_f32 v131, v115\n\t"
  "v_cvt_pk_bf16_f32 v132, v128, v129\n\t"
  "v_mfma_f32_32x32x16_bf16 v[236:251], v[152:155], v[136:139], v[236:251]\n\t"
  "ds_write_b128 v43, v[228:231] offset:2048\n\t"
  "v_exp_f32 v128, v116\n\t"
  "v_exp_f32 v129, v117\n\t"
  "v_cvt_pk_bf16_f32 v133, v130, v131\n\t"
  "v_exp_f32 v130, v118\n\t"
  "v_exp_f32 v131, v119\n\t"
  "v_cvt_pk_bf16_f32 v134, v128, v129\n\t"
  "ds_write_b128 v43, v[232:235] offset:3072\n\t"
  "v_exp_f32 v128, v120\n\t"
  "v_exp_f32 v129, v121\n\t"
  "v_cvt_pk_bf16_f32 v135, v130, v131\n\t"
  "v_exp_f32 v130, v122\n\t"
  "v_exp_f32 v131, v123\n\t"
  "v_cvt_pk_bf16_f32 v136, v128, v129\n\t"
  "v_mfma_f32_32x32x16_bf16 v[16:31], v[148:151], v[132:135], v[16:31]\n\t"
  "ds_write_b128 v44, v[236:239] offset:0\n\t"
  "v_exp_f32 v128, v124\n\t"
  "v_exp_f32 v129, v125\n\t"
  "v_cvt_pk_bf16_f32 v137, v130, v131\n\t"
  "v_exp_f32 v130, v126\n\t"
  "v_exp_f32 v131, v127\n\t"
  "v_cvt_pk_bf16_f32 v138, v128, v129\n\t"
  "ds_write_b128 v44, v[240:243] offset:1024\n\t"
  "v_cvt_pk_bf16_f32 v139, v130, v131\n\t"
  "ds_write_b128 v44, v[244:247] offset:2048\n\t"
  "ds_write_b128 v44, v[248:251] offset:3072\n\t"
  "v_mfma_f32_32x32x16_bf16 v[16:31], v[152:155], v[136:139], v[16:31]\n\t"
  "s_waitcnt lgkmcnt(0)\n\t"
  "s_mov_b64 exec, 1\n\t"
  "ds_add_u32 v54, v55 offset:0\n\t"
  "s_mov_b64 exec, -1\n\t"
  "ds_read_b96 v[252:254], v54 offset:0\n\t"
  "ds_read_b128 v[140:143], v45 offset:0\n\t"
  "ds_read_b128 v[144:147], v45 offset:1024\n\t"
  "ds_read_b128 v[148:151], v45 offset:2048\n\t"
  "ds_read_b128 v[152:155], v45 offset:3072\n\t"
  "ds_read_b128 v[156:159], v45 offset:4096\n\t"
  "ds_read_b128 v[160:163], v45 offset:5120\n\t"
  "ds_read_b128 v[164:167], v45 offset:6144\n\t"
  "ds_read_b128 v[168:171], v45 offset:7168\n\t"
  "ds_read_b128 v[172:175], v45 offset:8192\n\t"
  "ds_read_b128 v[176:179], v45 offset:9216\n\t"
  "ds_read_b128 v[180:183], v45 offset:10240\n\t"
  "ds_read_b128 v[184:187], v45 offset:11264\n\t"
  "s_waitcnt lgkmcnt(12)\n\t"
  "v_lshlrev_b32 v254, 2, v254\n\t"
  "v_min_u32 v8, v252, v254\n\t"
  "s_nop 0\n\t"
  "v_readfirstlane_b32 s52, v8\n\t"
  "s_nop 0\n\t"
  "s_cmp_lt_u32 s52, 4\n\t"
  "s_cbranch_scc0 LW2E_%=\n\t"
  "LW2_%=:\n\t"
  "ds_read_b96 v[252:254], v54 offset:0\n\t"
  "s_waitcnt lgkmcnt(0)\n\t"
  "v_lshlrev_b32 v254, 2, v254\n\t"
  "v_min_u32 v8, v252, v254\n\t"
  "s_nop 0\n\t"
  "v_readfirstlane_b32 s52, v8\n\t"
  "s_nop 0\n\t"
  "s_cmp_lt_u32 s52, 4\n\t"
  "s_cbranch_scc1 LW2_%=\n\t"
  "ds_read_b128 v[140:143], v45 offset:0\n\t"
  "ds_read_b128 v[144:147], v45 offset:1024\n\t"
  "ds_read_b128 v[148:151], v45 offset:2048\n\t"
  "ds_read_b128 v[152:155], v45 offset:3072\n\t"
  "ds_read_b128 v[156:159], v45 offset:4096\n\t"
  "ds_read_b128 v[160:163], v45 offset:5120\n\t"
  "ds_read_b128 v[164:167], v45 offset:6144\n\t"
  "ds_read_b128 v[168:171], v45 offset:7168\n\t"
  "ds_read_b128 v[172:175], v45 offset:8192\n\t"
  "ds_read_b128 v[176:179], v45 offset:9216\n\t"
  "ds_read_b128 v[180:183], v45 offset:10240\n\t"
  "ds_read_b128 v[184:187], v45 offset:11264\n\t"
  "LW2E_%=:\n\t"
  "s_mov_b32 s52, 0xffff0000\n\t"
  "s_mov_b32 s53, 0xffff0000\n\t"
  "v_cndmask_b32 v15, v253, 1.0, s[52:53]\n\t"
  "v_cmp_eq_u32 s[54:55], v13, v56\n\t"
  "v_mul_f32 v15, v15, v12\n\t"
  "v_cmp_le_f32 vcc, 0x2b800000, v12\n\t"
  "v_cmp_le_f32 s[56:57], v15, v62\n\t"
  "s_and_b64 s[52:53], s[52:53], vcc\n\t"
  "s_and_b64 s[54:55], s[54:55], s[56:57]\n\t"
  "s_cmp_eq_u64 s[52:53], 0\n\t"
  "s_cselect_b32 s51, 1, 0\n\t"
  "s_cmp_lg_u64 s[54:55], -1\n\t"
  "s_cselect_b32 s51, 1, s51\n\t"
  "s_waitcnt lgkmcnt(8)\n\t"
  "v_pk_add_f32 v[16:17], v[16:17], v[140:141]\n\t"
  "v_pk_add_f32 v[18:19], v[18:19], v[142:143]\n\t"
  "v_pk_add_f32 v[20:21], v[20:21], v[144:145]\n\t"
  "v_pk_add_f32 v[22:23], v[22:23], v[146:147]\n\t"
  "v_pk_add_f32 v[24:25], v[24:25], v[148:149]\n\t"
  "v_pk_add_f32 v[26:27], v[26:27], v[150:151]\n\t"
  "v_pk_add_f32 v[28:29], v[28:29], v[152:153]\n\t"
  "v_pk_add_f32 v[30:31], v[30:31], v[154:155]\n\t"
  "s_waitcnt lgkmcnt(4)\n\t"
  "v_pk_add_f32 v[16:17], v[16:17], v[156:157]\n\t"
  "v_pk_add_f32 v[18:19], v[18:19], v[158:159]\n\t"
  "v_pk_add_f32 v[20:21], v[20:21], v[160:161]\n\t"
  "v_pk_add_f32 v[22:23], v[22:23], v[162:163]\n\t"
  "v_pk_add_f32 v[24:25], v[24:25], v[164:165]\n\t"
  "v_pk_add_f32 v[26:27], v[26:27], v[166:167]\n\t"
  "v_pk_add_f32 v[28:29], v[28:29], v[168:169]\n\t"
  "v_pk_add_f32 v[30:31], v[30:31], v[170:171]\n\t"
  "s_waitcnt lgkmcnt(0)\n\t"
  "v_pk_add_f32 v[16:17], v[16:17], v[172:173]\n\t"
  "v_pk_add_f32 v[18:19], v[18:19], v[174:175]\n\t"
  "v_pk_add_f32 v[20:21], v[20:21], v[176:177]\n\t"
  "v_pk_add_f32 v[22:23], v[22:23], v[178:179]\n\t"
  "v_pk_add_f32 v[24:25], v[24:25], v[180:181]\n\t"
  "v_pk_add_f32 v[26:27], v[26:27], v[182:183]\n\t"
  "v_pk_add_f32 v[28:29], v[28:29], v[184:185]\n\t"
  "v_pk_add_f32 v[30:31], v[30:31], v[186:187]\n\t"
  "v_mov_b32 v14, v28\n\t"
  "v_mov_b32 v15, v28\n\t"
  "s_nop 1\n\t"
  "v_permlane32_swap_b32 v14, v15\n\t"
  "s_nop 0\n\t"
  "v_rcp_f32 v14, v15\n\t"
  "s_nop 0\n\t"
  "v_pk_mul_f32 v[16:17], v[16:17], v[14:15] op_sel_hi:[1,0]\n\t"
  "v_pk_mul_f32 v[18:19], v[18:19], v[14:15] op_sel_hi:[1,0]\n\t"
  "v_pk_mul_f32 v[20:21], v[20:21], v[14:15] op_sel_hi:[1,0]\n\t"
  "v_pk_mul_f32 v[22:23], v[22:23], v[14:15] op_sel_hi:[1,0]\n\t"
  "v_pk_mul_f32 v[24:25], v[24:25], v[14:15] op_sel_hi:[1,0]\n\t"
  "v_pk_mul_f32 v[26:27], v[26:27], v[14:15] op_sel_hi:[1,0]\n\t"
  "v_pk_mul_f32 v[28:29], v[28:29], v[14:15] op_sel_hi:[1,0]\n\t"
  "v_pk_mul_f32 v[30:31], v[30:31], v[14:15] op_sel_hi:[1,0]\n\t"
  "v_cvt_pk_bf16_f32 v64, v16, v17\n\t"
  "v_cvt_pk_bf16_f32 v65, v18, v19\n\t"
  "v_cvt_pk_bf16_f32 v66, v20, v21\n\t"
  "v_cvt_pk_bf16_f32 v67, v22, v23\n\t"
  "v_cvt_pk_bf16_f32 v68, v24, v25\n\t"
  "v_cvt_pk_bf16_f32 v69, v26, v27\n\t"
  "v_cvt_pk_bf16_f32 v70, v28, v29\n\t"
  "v_cvt_pk_bf16_f32 v71, v30, v31\n\t"
  "s_cmp_lg_u32 s51, 0\n\t"
  "v_cndmask_b32 v70, 0, v70, s[60:61]\n\t"
  "v_cndmask_b32 v71, 0, v71, s[60:61]\n\t"
  "v_min_u32 v65, -2, v65\n\t"
  "v_min_u32 v67, -2, v67\n\t"
  "v_min_u32 v69, -2, v69\n\t"
  "v_min_u32 v71, -2, v71\n\t"
  "s_cbranch_scc1 LB_%=\n\t"
  "buffer_store_dwordx4 v[64:67], v39, s[36:39], s66 offen sc1\n\t"
  "buffer_store_dwordx4 v[68:71], v39, s[36:39], s66 offen offset:1024 sc1\n\t"
  "s_nop 0\n\t"
  "LB_%=:\n\t"
  "s_nop 0\n\t"
      : "+{v[32:47]}"(va), "+{v[48:63]}"(vb), "=&{s51}"(bad), "+{v[0:7]}"(ta)
      : "{s[36:39]}"(rw), "{s[40:43]}"(rkv), "{s45}"(d0), "{s46}"(d0 + 262144), "{s47}"((t & 7) * 8192 + (h & 1) * 4096), "{s50}"(isv), "{s66}"(oo),
        "{s72}"(ol), "{s73}"(oc), "{s74}"(rr), "{s[76:79]}"(rout)
      : "v8", "v9", "v12", "v13", "v14", "v15", "v16", "v17", "v18", "v19", "v20", "v21", "v22", "v23", "v24", "v25", "v26", "v27", "v28", "v29", "v30", "v31", "v64", "v65", "v66", "v67", "v68", "v69", "v70", "v71", "v72", "v73", "v74", "v75", "v76", "v77", "v78", "v79", "v80", "v81", "v82", "v83", "v84", "v85", "v86", "v87", "v88", "v89", "v90", "v91", "v92", "v93", "v94", "v95", "v96", "v97", "v98", "v99", "v100", "v101", "v102", "v103", "v104", "v105", "v106", "v107", "v108", "v109", "v110", "v111", "v112", "v113", "v114", "v115", "v116", "v117", "v118", "v119", "v120", "v121", "v122", "v123", "v124", "v125", "v126", "v127", "v128", "v129", "v130", "v131", "v132", "v133", "v134", "v135", "v136", "v137", "v138", "v139", "v140", "v141", "v142", "v143", "v144", "v145", "v146", "v147", "v148", "v149", "v150", "v151", "v152", "v153", "v154", "v155", "v156", "v157", "v158", "v159", "v160", "v161", "v162", "v163", "v164", "v165", "v166", "v167", "v168", "v169", "v170", "v171", "v172", "v173", "v174", "v175", "v176", "v177", "v178", "v179", "v180", "v181", "v182", "v183", "v184", "v185", "v186", "v187", "v188", "v189", "v190", "v191", "v192", "v193", "v194", "v195", "v196", "v197", "v198", "v199", "v200", "v201", "v202", "v203", "v204", "v205", "v206", "v207", "v208", "v209", "v210", "v211", "v212", "v213", "v214", "v215", "v216", "v217", "v218", "v219", "v220", "v221", "v222", "v223", "v224", "v225", "v226", "v227", "v228", "v229", "v230", "v231", "v232", "v233", "v234", "v235", "v236", "v237", "v238", "v239", "v240", "v241", "v242", "v243", "v244", "v245", "v246", "v247", "v248", "v249", "v250", "v251", "v252", "v253", "v254", "v255", "s52", "s53", "s54", "s55", "s56", "s57", "s58", "s59", "s60", "s61", "vcc", "scc", "memory"
  );
  if (__builtin_expect(__builtin_amdgcn_readfirstlane(bad), 0)) slow_attention(lds, rkv, rw, oo, w, fresh_lane());
  asm volatile(
  "s_cmp_gt_u32 s74, 27\n\t"
  "s_cbranch_scc1 LE_%=\n\t"
  "s_add_u32 s54, s72, 4096\n\t"
  "s_add_u32 s55, s72, 8192\n\t"
  "s_add_u32 s56, s72, 12288\n\t"
  "v_xor_b32 v8, 64, v1\n\t"
  "v_xor_b32 v9, 64, v2\n\t"
  "v_xor_b32 v12, 0x2000, v3\n\t"
  "v_and_b32 v13, -32, v5\n\t"
  "LW3_%=:\n\t"
  "ds_read_b32 v7, v13 offset:44\n\t"
  "s_waitcnt lgkmcnt(0)\n\t"
  "v_readfirstlane_b32 s52, v7\n\t"
  "s_nop 0\n\t"
  "s_cmp_lt_u32 s52, 4\n\t"
  "s_cbranch_scc1 LW3_%=\n\t"
  "ds_read_b64_tr_b16 v[128:129], v1 offset:0\n\t"
  "ds_read_b64_tr_b16 v[130:131], v1 offset:1024\n\t"
  "ds_read_b64_tr_b16 v[132:133], v8 offset:0\n\t"
  "ds_read_b64_tr_b16 v[134:135], v8 offset:1024\n\t"
  "ds_read_b64_tr_b16 v[136:137], v1 offset:2048\n\t"
  "ds_read_b64_tr_b16 v[138:139], v2 offset:3072\n\t"
  "ds_read_b64_tr_b16 v[140:141], v8 offset:2048\n\t"
  "ds_read_b64_tr_b16 v[142:143], v9 offset:3072\n\t"
  "ds_read_b64_tr_b16 v[144:145], v1 offset:3584\n\t"
  "ds_read_b64_tr_b16 v[146:147], v1 offset:4608\n\t"
  "ds_read_b64_tr_b16 v[148:149], v8 offset:3584\n\t"
  "ds_read_b64_tr_b16 v[150:151], v8 offset:4608\n\t"
  "ds_read_b64_tr_b16 v[152:153], v1 offset:5632\n\t"
  "ds_read_b64_tr_b16 v[154:155], v2 offset:6656\n\t"
  "ds_read_b64_tr_b16 v[156:157], v8 offset:5632\n\t"
  "ds_read_b64_tr_b16 v[158:159], v9 offset:6656\n\t"
  "ds_read_b64_tr_b16 v[160:161], v1 offset:7168\n\t"
  "ds_read_b64_tr_b16 v[162:163], v1 offset:8192\n\t"
  "ds_read_b64_tr_b16 v[164:165], v8 offset:7168\n\t"
  "ds_read_b64_tr_b16 v[166:167], v8 offset:8192\n\t"
  "ds_read_b64_tr_b16 v[168:169], v1 offset:9216\n\t"
  "ds_read_b64_tr_b16 v[170:171], v2 offset:10240\n\t"
  "ds_read_b64_tr_b16 v[172:173], v8 offset:9216\n\t"
  "ds_read_b64_tr_b16 v[174:175], v9 offset:10240\n\t"
  "ds_read_b64_tr_b16 v[176:177], v1 offset:10752\n\t"
  "ds_read_b64_tr_b16 v[178:179], v1 offset:11776\n\t"
  "ds_read_b64_tr_b16 v[180:181], v8 offset:10752\n\t"
  "ds_read_b64_tr_b16 v[182:183], v8 offset:11776\n\t"
  "ds_read_b64_tr_b16 v[184:185], v1 offset:12800\n\t"
  "ds_read_b64_tr_b16 v[186:187], v2 offset:13824\n\t"
  "ds_read_b64_tr_b16 v[188:189], v8 offset:12800\n\t"
  "ds_read_b64_tr_b16 v[190:191], v9 offset:13824\n\t"
  "ds_read_b64_tr_b16 v[192:193], v1 offset:14336\n\t"
  "ds_read_b64_tr_b16 v[194:195], v1 offset:15360\n\t"
  "ds_read_b64_tr_b16 v[196:197], v8 offset:14336\n\t"
  "ds_read_b64_tr_b16 v[198:199], v8 offset:15360\n\t"
  "ds_read_b64_tr_b16 v[200:201], v1 offset:16384\n\t"
  "ds_read_b64_tr_b16 v[202:203], v2 offset:17408\n\t"
  "ds_read_b64_tr_b16 v[204:205], v8 offset:16384\n\t"
  "ds_read_b64_tr_b16 v[206:207], v9 offset:17408\n\t"
  "buffer_load_dwordx4 v[64:67], v0, s[36:39], s72 offen offset:0 sc1\n\t"
  "buffer_load_dwordx4 v[68:71], v0, s[36:39], s72 offen offset:1024 sc1\n\t"
  "buffer_load_dwordx4 v[72:75], v0, s[36:39], s72 offen offset:2048 sc1\n\t"
  "buffer_load_dwordx4 v[76:79], v0, s[36:39], s72 offen offset:3072 sc1\n\t"
  "buffer_load_dwordx4 v[80:83], v0, s[36:39], s54 offen offset:0 sc1\n\t"
  "buffer_load_dwordx4 v[84:87], v0, s[36:39], s54 offen offset:1024 sc1\n\t"
  "buffer_load_dwordx4 v[88:91], v0, s[36:39], s54 offen offset:2048 sc1\n\t"
  "buffer_load_dwordx4 v[92:95], v0, s[36:39], s54 offen offset:3072 sc1\n\t"
  "buffer_load_dwordx4 v[96:99], v0, s[36:39], s55 offen offset:0 sc1\n\t"
  "buffer_load_dwordx4 v[100:103], v0, s[36:39], s55 offen offset:1024 sc1\n\t"
  "buffer_load_dwordx4 v[104:107], v0, s[36:39], s55 offen offset:2048 sc1\n\t"
  "buffer_load_dwordx4 v[108:111], v0, s[36:39], s55 offen offset:3072 sc1\n\t"
  "buffer_load_dwordx4 v[112:115], v0, s[36:39], s56 offen offset:0 sc1\n\t"
  "buffer_load_dwordx4 v[116:119], v0, s[36:39], s56 offen offset:1024 sc1\n\t"
  "buffer_load_dwordx4 v[120:123], v0, s[36:39], s56 offen offset:2048 sc1\n\t"
  "buffer_load_dwordx4 v[124:127], v0, s[36:39], s56 offen offset:3072 sc1\n\t"
  "ds_read_b64_tr_b16 v[208:209], v1 offset:17920\n\t"
  "ds_read_b64_tr_b16 v[210:211], v1 offset:18944\n\t"
  "ds_read_b64_tr_b16 v[212:213], v8 offset:17920\n\t"
  "ds_read_b64_tr_b16 v[214:215], v8 offset:18944\n\t"
  "ds_read_b64_tr_b16 v[216:217], v1 offset:19968\n\t"
  "ds_read_b64_tr_b16 v[218:219], v2 offset:20992\n\t"
  "ds_read_b64_tr_b16 v[220:221], v8 offset:19968\n\t"
  "ds_read_b64_tr_b16 v[222:223], v9 offset:20992\n\t"
  "ds_read_b64_tr_b16 v[224:225], v1 offset:21504\n\t"
  "ds_read_b64_tr_b16 v[226:227], v1 offset:22528\n\t"
  "ds_read_b64_tr_b16 v[228:229], v8 offset:21504\n\t"
  "ds_read_b64_tr_b16 v[230:231], v8 offset:22528\n\t"
  "ds_read_b64_tr_b16 v[232:233], v1 offset:23552\n\t"
  "ds_read_b64_tr_b16 v[234:235], v2 offset:24576\n\t"
  "ds_read_b64_tr_b16 v[236:237], v8 offset:23552\n\t"
  "ds_read_b64_tr_b16 v[238:239], v9 offset:24576\n\t"
  "ds_read_b64_tr_b16 v[240:241], v1 offset:25088\n\t"
  "ds_read_b64_tr_b16 v[242:243], v1 offset:26112\n\t"
  "ds_read_b64_tr_b16 v[244:245], v8 offset:25088\n\t"
  "ds_read_b64_tr_b16 v[246:247], v8 offset:26112\n\t"
  "ds_read_b64_tr_b16 v[248:249], v1 offset:27136\n\t"
  "ds_read_b64_tr_b16 v[250:251], v2 offset:28160\n\t"
  "ds_read_b64_tr_b16 v[252:253], v8 offset:27136\n\t"
  "ds_read_b64_tr_b16 v[254:255], v9 offset:28160\n\t"
  "s_mov_b32 s53, 0x20000\n\t"
  "s_waitcnt lgkmcnt(0)\n\t"
  "s_waitcnt vmcnt(14)\n\t"
  "LC0_%=:\n\t"
  "v_max3_u32 v7, v65, v67, v69\n\t"
  "v_max_u32 v7, v71, v7\n\t"
  "v_cmp_eq_u32 vcc, -1, v7\n\t"
  "s_cbranch_vccnz LR0_%=\n\t"
  "LD0_%=:\n\t"
  "v_mfma_f32_32x32x16_bf16 v[32:47], v[128:131], v[64:67], 0\n\t"
  "v_mfma_f32_32x32x16_bf16 v[48:63], v[132:135], v[64:67], 0\n\t"
  "v_mfma_f32_32x32x16_bf16 v[32:47], v[136:139], v[68:71], v[32:47]\n\t"
  "v_mfma_f32_32x32x16_bf16 v[48:63], v[140:143], v[68:71], v[48:63]\n\t"
  "s_waitcnt vmcnt(12)\n\t"
  "LC1_%=:\n\t"
  "v_max3_u32 v7, v73, v75, v77\n\t"
  "v_max_u32 v7, v79, v7\n\t"
  "v_cmp_eq_u32 vcc, -1, v7\n\t"
  "s_cbranch_vccnz LR1_%=\n\t"
  "LD1_%=:\n\t"
  "v_mfma_f32_32x32x16_bf16 v[32:47], v[144:147], v[72:75], v[32:47]\n\t"
  "v_mfma_f32_32x32x16_bf16 v[48:63], v[148:151], v[72:75], v[48:63]\n\t"
  "v_mfma_f32_32x32x16_bf16 v[32:47], v[152:155], v[76:79], v[32:47]\n\t"
  "v_mfma_f32_32x32x16_bf16 v[48:63], v[156:159], v[76:79], v[48:63]\n\t"
  "s_waitcnt vmcnt(10)\n\t"
  "LC2_%=:\n\t"
  "v_max3_u32 v7, v81, v83, v85\n\t"
  "v_max_u32 v7, v87, v7\n\t"
  "v_cmp_eq_u32 vcc, -1, v7\n\t"
  "s_cbranch_vccnz LR2_%=\n\t"
  "LD2_%=:\n\t"
  "v_mfma_f32_32x32x16_bf16 v[32:47], v[160:163], v[80:83], v[32:47]\n\t"
  "v_mfma_f32_32x32x16_bf16 v[48:63], v[164:167], v[80:83], v[48:63]\n\t"
  "v_mfma_f32_32x32x16_bf16 v[32:47], v[168:171], v[84:87], v[32:47]\n\t"
  "v_mfma_f32_32x32x16_bf16 v[48:63], v[172:175], v[84:87], v[48:63]\n\t"
  "s_waitcnt vmcnt(8)\n\t"
  "LC3_%=:\n\t"
  "v_max3_u32 v7, v89, v91, v93\n\t"
  "v_max_u32 v7, v95, v7\n\t"
  "v_cmp_eq_u32 vcc, -1, v7\n\t"
  "s_cbranch_vccnz LR3_%=\n\t"
  "LD3_%=:\n\t"
  "v_mfma_f32_32x32x16_bf16 v[32:47], v[176:179], v[88:91], v[32:47]\n\t"
  "v_mfma_f32_32x32x16_bf16 v[48:63], v[180:183], v[88:91], v[48:63]\n\t"
  "v_mfma_f32_32x32x16_bf16 v[32:47], v[184:187], v[92:95], v[32:47]\n\t"
  "v_mfma_f32_32x32x16_bf16 v[48:63], v[188:191], v[92:95], v[48:63]\n\t"
  "s_waitcnt vmcnt(6)\n\t"
  "LC4_%=:\n\t"
  "v_max3_u32 v7, v97, v99, v101\n\t"
  "v_max_u32 v7, v103, v7\n\t"
  "v_cmp_eq_u32 vcc, -1, v7\n\t"
  "s_cbranch_vccnz LR4_%=\n\t"
  "LD4_%=:\n\t"
  "v_mfma_f32_32x32x16_bf16 v[32:47], v[192:195], v[96:99], v[32:47]\n\t"
  "v_mfma_f32_32x32x16_bf16 v[48:63], v[196:199], v[96:99], v[48:63]\n\t"
  "v_mfma_f32_32x32x16_bf16 v[32:47], v[200:203], v[100:103], v[32:47]\n\t"
  "v_mfma_f32_32x32x16_bf16 v[48:63], v[204:207], v[100:103], v[48:63]\n\t"
  "s_waitcnt vmcnt(4)\n\t"
  "LC5_%=:\n\t"
  "v_max3_u32 v7, v105, v107, v109\n\t"
  "v_max_u32 v7, v111, v7\n\t"
  "v_cmp_eq_u32 vcc, -1, v7\n\t"
  "s_cbranch_vccnz LR5_%=\n\t"
  "LD5_%=:\n\t"
  "v_mfma_f32_32x32x16_bf16 v[32:47], v[208:211], v[104:107], v[32:47]\n\t"
  "v_mfma_f32_32x32x16_bf16 v[48:63], v[212:215], v[104:107], v[48:63]\n\t"
  "v_mfma_f32_32x32x16_bf16 v[32:47], v[216:219], v[108:111], v[32:47]\n\t"
  "v_mfma_f32_32x32x16_bf16 v[48:63], v[220:223], v[108:111], v[48:63]\n\t"
  "s_waitcnt vmcnt(2)\n\t"
  "LC6_%=:\n\t"
  "v_max3_u32 v7, v113, v115, v117\n\t"
  "v_max_u32 v7, v119, v7\n\t"
  "v_cmp_eq_u32 vcc, -1, v7\n\t"
  "s_cbranch_vccnz LR6_%=\n\t"
  "LD6_%=:\n\t"
  "v_mfma_f32_32x32x16_bf16 v[32:47], v[224:227], v[112:115], v[32:47]\n\t"
  "v_mfma_f32_32x32x16_bf16 v[48:63], v[228:231], v[112:115], v[48:63]\n\t"
  "v_mfma_f32_32x32x16_bf16 v[32:47], v[232:235], v[116:119], v[32:47]\n\t"
  "v_mfma_f32_32x32x16_bf16 v[48:63], v[236:239], v[116:119], v[48:63]\n\t"
  "s_waitcnt vmcnt(0)\n\t"
  "LC7_%=:\n\t"
  "v_max3_u32 v7, v121, v123, v125\n\t"
  "v_max_u32 v7, v127, v7\n\t"
  "v_cmp_eq_u32 vcc, -1, v7\n\t"
  "s_cbranch_vccnz LR7_%=\n\t"
  "LD7_%=:\n\t"
  "v_mfma_f32_32x32x16_bf16 v[32:47], v[240:243], v[120:123], v[32:47]\n\t"
  "v_mfma_f32_32x32x16_bf16 v[48:63], v[244:247], v[120:123], v[48:63]\n\t"
  "v_mfma_f32_32x32x16_bf16 v[48:63], v[252:255], v[124:127], v[48:63]\n\t"
  "v_mfma_f32_32x32x16_bf16 v[32:47], v[248:251], v[124:127], v[32:47]\n\t"
  "s_nop 10\n\t"
  "ds_write_b128 v3, v[48:51] offset:0\n\t"
  "ds_write_b128 v3, v[52:55] offset:1024\n\t"
  "ds_write_b128 v3, v[56:59] offset:2048\n\t"
  "ds_write_b128 v3, v[60:63] offset:3072\n\t"
  "s_waitcnt lgkmcnt(0)\n\t"
  "s_mov_b64 exec, 1\n\t"
  "ds_add_u32 v5, v6\n\t"
  "s_mov_b64 exec, -1\n\t"
  "ds_read_b32 v7, v5\n\t"
  "ds_read_b128 v[64:67], v12 offset:0\n\t"
  "ds_read_b128 v[68:71], v12 offset:1024\n\t"
  "ds_read_b128 v[72:75], v12 offset:2048\n\t"
  "ds_read_b128 v[76:79], v12 offset:3072\n\t"
  "s_waitcnt lgkmcnt(4)\n\t"
  "v_readfirstlane_b32 s52, v7\n\t"
  "s_nop 0\n\t"
  "s_cmp_lt_u32 s52, 2\n\t"
  "s_cbranch_scc0 LW5E_%=\n\t"
  "LW5_%=:\n\t"
  "ds_read_b32 v7, v5\n\t"
  "s_waitcnt lgkmcnt(0)\n\t"
  "v_readfirstlane_b32 s52, v7\n\t"
  "s_nop 0\n\t"
  "s_cmp_lt_u32 s52, 2\n\t"
  "s_cbranch_scc1 LW5_%=\n\t"
  "ds_read_b128 v[64:67], v12 offset:0\n\t"
  "ds_read_b128 v[68:71], v12 offset:1024\n\t"
  "ds_read_b128 v[72:75], v12 offset:2048\n\t"
  "ds_read_b128 v[76:79], v12 offset:3072\n\t"
  "LW5E_%=:\n\t"
  "s_waitcnt lgkmcnt(2)\n\t"
  "v_pk_add_f32 v[32:33], v[32:33], v[64:65]\n\t"
  "v_pk_add_f32 v[34:35], v[34:35], v[66:67]\n\t"
  "v_pk_add_f32 v[36:37], v[36:37], v[68:69]\n\t"
  "v_pk_add_f32 v[38:39], v[38:39], v[70:71]\n\t"
  "v_cvt_pk_bf16_f32 v80, v32, v33\n\t"
  "v_cvt_pk_bf16_f32 v81, v34, v35\n\t"
  "v_cvt_pk_bf16_f32 v82, v36, v37\n\t"
  "v_cvt_pk_bf16_f32 v83, v38, v39\n\t"
  "buffer_store_dwordx4 v[80:83], v4, s[76:79], s73 offen offset:0\n\t"
  "s_waitcnt lgkmcnt(0)\n\t"
  "v_pk_add_f32 v[40:41], v[40:41], v[72:73]\n\t"
  "v_pk_add_f32 v[42:43], v[42:43], v[74:75]\n\t"
  "v_pk_add_f32 v[44:45], v[44:45], v[76:77]\n\t"
  "v_pk_add_f32 v[46:47], v[46:47], v[78:79]\n\t"
  "v_cvt_pk_bf16_f32 v84, v40, v41\n\t"
  "v_cvt_pk_bf16_f32 v85, v42, v43\n\t"
  "v_cvt_pk_bf16_f32 v86, v44, v45\n\t"
  "v_cvt_pk_bf16_f32 v87, v46, v47\n\t"
  "buffer_store_dwordx4 v[84:87], v4, s[76:79], s73 offen offset:32\n\t"
  "s_endpgm\n\t"
  "LR0_%=:\n\t"
  "s_sub_u32 s53, s53, 1\n\t"
  "s_cbranch_scc1 LD0_%=\n\t"
  "s_waitcnt vmcnt(0)\n\t"
  "buffer_load_dwordx4 v[64:67], v0, s[36:39], s72 offen offset:0 sc1\n\t"
  "buffer_load_dwordx4 v[68:71], v0, s[36:39], s72 offen offset:1024 sc1\n\t"
  "v_max3_u32 v7, v73, v75, v77\n\t"
  "v_max_u32 v7, v79, v7\n\t"
  "v_cmp_eq_u32 vcc, -1, v7\n\t"
  "s_cbranch_vccz LS01_%=\n\t"
  "buffer_load_dwordx4 v[72:75], v0, s[36:39], s72 offen offset:2048 sc1\n\t"
  "buffer_load_dwordx4 v[76:79], v0, s[36:39], s72 offen offset:3072 sc1\n\t"
  "LS01_%=:\n\t"
  "v_max3_u32 v7, v81, v83, v85\n\t"
  "v_max_u32 v7, v87, v7\n\t"
  "v_cmp_eq_u32 vcc, -1, v7\n\t"
  "s_cbranch_vccz LS02_%=\n\t"
  "buffer_load_dwordx4 v[80:83], v0, s[36:39], s54 offen offset:0 sc1\n\t"
  "buffer_load_dwordx4 v[84:87], v0, s[36:39], s54 offen offset:1024 sc1\n\t"
  "LS02_%=:\n\t"
  "v_max3_u32 v7, v89, v91, v93\n\t"
  "v_max_u32 v7, v95, v7\n\t"
  "v_cmp_eq_u32 vcc, -1, v7\n\t"
  "s_cbranch_vccz LS03_%=\n\t"
  "buffer_load_dwordx4 v[88:91], v0, s[36:39], s54 offen offset:2048 sc1\n\t"
  "buffer_load_dwordx4 v[92:95], v0, s[36:39], s54 offen offset:3072 sc1\n\t"
  "LS03_%=:\n\t"
  "v_max3_u32 v7, v97, v99, v101\n\t"
  "v_max_u32 v7, v103, v7\n\t"
  "v_cmp_eq_u32 vcc, -1, v7\n\t"
  "s_cbranch_vccz LS04_%=\n\t"
  "buffer_load_dwordx4 v[96:99], v0, s[36:39], s55 offen offset:0 sc1\n\t"
  "buffer_load_dwordx4 v[100:103], v0, s[36:39], s55 offen offset:1024 sc1\n\t"
  "LS04_%=:\n\t"
  "v_max3_u32 v7, v105, v107, v109\n\t"
  "v_max_u32 v7, v111, v7\n\t"
  "v_cmp_eq_u32 vcc, -1, v7\n\t"
  "s_cbranch_vccz LS05_%=\n\t"
  "buffer_load_dwordx4 v[104:107], v0, s[36:39], s55 offen offset:2048 sc1\n\t"
  "buffer_load_dwordx4 v[108:111], v0, s[36:39], s55 offen offset:3072 sc1\n\t"
  "LS05_%=:\n\t"
  "v_max3_u32 v7, v113, v115, v117\n\t"
  "v_max_u32 v7, v119, v7\n\t"
  "v_cmp_eq_u32 vcc, -1, v7\n\t"
  "s_cbranch_vccz LS06_%=\n\t"
  "buffer_load_dwordx4 v[112:115], v0, s[36:39], s56 offen offset:0 sc1\n\t"
  "buffer_load_dwordx4 v[116:119], v0, s[36:39], s56 offen offset:1024 sc1\n\t"
  "LS06_%=:\n\t"
  "v_max3_u32 v7, v121, v123, v125\n\t"
  "v_max_u32 v7, v127, v7\n\t"
  "v_cmp_eq_u32 vcc, -1, v7\n\t"
  "s_cbranch_vccz LS07_%=\n\t"
  "buffer_load_dwordx4 v[120:123], v0, s[36:39], s56 offen offset:2048 sc1\n\t"
  "buffer_load_dwordx4 v[124:127], v0, s[36:39], s56 offen offset:3072 sc1\n\t"
  "LS07_%=:\n\t"
  "s_waitcnt vmcnt(0)\n\t"
  "s_branch LC0_%=\n\t"
  "LR1_%=:\n\t"
  "s_sub_u32 s53, s53, 1\n\t"
  "s_cbranch_scc1 LD1_%=\n\t"
  "s_waitcnt vmcnt(0)\n\t"
  "buffer_load_dwordx4 v[72:75], v0, s[36:39], s72 offen offset:2048 sc1\n\t"
  "buffer_load_dwordx4 v[76:79], v0, s[36:39], s72 offen offset:3072 sc1\n\t"
  "v_max3_u32 v7, v81, v83, v85\n\t"
  "v_max_u32 v7, v87, v7\n\t"
  "v_cmp_eq_u32 vcc, -1, v7\n\t"
  "s_cbranch_vccz LS12_%=\n\t"
  "buffer_load_dwordx4 v[80:83], v0, s[36:39], s54 offen offset:0 sc1\n\t"
  "buffer_load_dwordx4 v[84:87], v0, s[36:39], s54 offen offset:1024 sc1\n\t"
  "LS12_%=:\n\t"
  "v_max3_u32 v7, v89, v91, v93\n\t"
  "v_max_u32 v7, v95, v7\n\t"
  "v_cmp_eq_u32 vcc, -1, v7\n\t"
  "s_cbranch_vccz LS13_%=\n\t"
  "buffer_load_dwordx4 v[88:91], v0, s[36:39], s54 offen offset:2048 sc1\n\t"
  "buffer_load_dwordx4 v[92:95], v0, s[36:39], s54 offen offset:3072 sc1\n\t"
  "LS13_%=:\n\t"
  "v_max3_u32 v7, v97, v99, v101\n\t"
  "v_max_u32 v7, v103, v7\n\t"
  "v_cmp_eq_u32 vcc, -1, v7\n\t"
  "s_cbranch_vccz LS14_%=\n\t"
  "buffer_load_dwordx4 v[96:99], v0, s[36:39], s55 offen offset:0 sc1\n\t"
  "buffer_load_dwordx4 v[100:103], v0, s[36:39], s55 offen offset:1024 sc1\n\t"
  "LS14_%=:\n\t"
  "v_max3_u32 v7, v105, v107, v109\n\t"
  "v_max_u32 v7, v111, v7\n\t"
  "v_cmp_eq_u32 vcc, -1, v7\n\t"
  "s_cbranch_vccz LS15_%=\n\t"
  "buffer_load_dwordx4 v[104:107], v0, s[36:39], s55 offen offset:2048 sc1\n\t"
  "buffer_load_dwordx4 v[108:111], v0, s[36:39], s55 offen offset:3072 sc1\n\t"
  "LS15_%=:\n\t"
  "v_max3_u32 v7, v113, v115, v117\n\t"
  "v_max_u32 v7, v119, v7\n\t"
  "v_cmp_eq_u32 vcc, -1, v7\n\t"
  "s_cbranch_vccz LS16_%=\n\t"
  "buffer_load_dwordx4 v[112:115], v0, s[36:39], s56 offen offset:0 sc1\n\t"
  "buffer_load_dwordx4 v[116:119], v0, s[36:39], s56 offen offset:1024 sc1\n\t"
  "LS16_%=:\n\t"
  "v_max3_u32 v7, v121, v123, v125\n\t"
  "v_max_u32 v7, v127, v7\n\t"
  "v_cmp_eq_u32 vcc, -1, v7\n\t"
  "s_cbranch_vccz LS17_%=\n\t"
  "buffer_load_dwordx4 v[120:123], v0, s[36:39], s56 offen offset:2048 sc1\n\t"
  "buffer_load_dwordx4 v[124:127], v0, s[36:39], s56 offen offset:3072 sc1\n\t"
  "LS17_%=:\n\t"
  "s_waitcnt vmcnt(0)\n\t"
  "s_branch LC1_%=\n\t"
  "LR2_%=:\n\t"
  "s_sub_u32 s53, s53, 1\n\t"
  "s_cbranch_scc1 LD2_%=\n\t"
  "s_waitcnt vmcnt(0)\n\t"
  "buffer_load_dwordx4 v[80:83], v0, s[36:39], s54 offen offset:0 sc1\n\t"
  "buffer_load_dwordx4 v[84:87], v0, s[36:39], s54 offen offset:1024 sc1\n\t"
  "v_max3_u32 v7, v89, v91, v93\n\t"
  "v_max_u32 v7, v95, v7\n\t"
  "v_cmp_eq_u32 vcc, -1, v7\n\t"
  "s_cbranch_vccz LS23_%=\n\t"
  "buffer_load_dwordx4 v[88:91], v0, s[36:39], s54 offen offset:2048 sc1\n\t"
  "buffer_load_dwordx4 v[92:95], v0, s[36:39], s54 offen offset:3072 sc1\n\t"
  "LS23_%=:\n\t"
  "v_max3_u32 v7, v97, v99, v101\n\t"
  "v_max_u32 v7, v103, v7\n\t"
  "v_cmp_eq_u32 vcc, -1, v7\n\t"
  "s_cbranch_vccz LS24_%=\n\t"
  "buffer_load_dwordx4 v[96:99], v0, s[36:39], s55 offen offset:0 sc1\n\t"
  "buffer_load_dwordx4 v[100:103], v0, s[36:39], s55 offen offset:1024 sc1\n\t"
  "LS24_%=:\n\t"
  "v_max3_u32 v7, v105, v107, v109\n\t"
  "v_max_u32 v7, v111, v7\n\t"
  "v_cmp_eq_u32 vcc, -1, v7\n\t"
  "s_cbranch_vccz LS25_%=\n\t"
  "buffer_load_dwordx4 v[104:107], v0, s[36:39], s55 offen offset:2048 sc1\n\t"
  "buffer_load_dwordx4 v[108:111], v0, s[36:39], s55 offen offset:3072 sc1\n\t"
  "LS25_%=:\n\t"
  "v_max3_u32 v7, v113, v115, v117\n\t"
  "v_max_u32 v7, v119, v7\n\t"
  "v_cmp_eq_u32 vcc, -1, v7\n\t"
  "s_cbranch_vccz LS26_%=\n\t"
  "buffer_load_dwordx4 v[112:115], v0, s[36:39], s56 offen offset:0 sc1\n\t"
  "buffer_load_dwordx4 v[116:119], v0, s[36:39], s56 offen offset:1024 sc1\n\t"
  "LS26_%=:\n\t"
  "v_max3_u32 v7, v121, v123, v125\n\t"
  "v_max_u32 v7, v127, v7\n\t"
  "v_cmp_eq_u32 vcc, -1, v7\n\t"
  "s_cbranch_vccz LS27_%=\n\t"
  "buffer_load_dwordx4 v[120:123], v0, s[36:39], s56 offen offset:2048 sc1\n\t"
  "buffer_load_dwordx4 v[124:127], v0, s[36:39], s56 offen offset:3072 sc1\n\t"
  "LS27_%=:\n\t"
  "s_waitcnt vmcnt(0)\n\t"
  "s_branch LC2_%=\n\t"
  "LR3_%=:\n\t"
  "s_sub_u32 s53, s53, 1\n\t"
  "s_cbranch_scc1 LD3_%=\n\t"
  "s_waitcnt vmcnt(0)\n\t"
  "buffer_load_dwordx4 v[88:91], v0, s[36:39], s54 offen offset:2048 sc1\n\t"
  "buffer_load_dwordx4 v[92:95], v0, s[36:39], s54 offen offset:3072 sc1\n\t"
  "v_max3_u32 v7, v97, v99, v101\n\t"
  "v_max_u32 v7, v103, v7\n\t"
  "v_cmp_eq_u32 vcc, -1, v7\n\t"
  "s_cbranch_vccz LS34_%=\n\t"
  "buffer_load_dwordx4 v[96:99], v0, s[36:39], s55 offen offset:0 sc1\n\t"
  "buffer_load_dwordx4 v[100:103], v0, s[36:39], s55 offen offset:1024 sc1\n\t"
  "LS34_%=:\n\t"
  "v_max3_u32 v7, v105, v107, v109\n\t"
  "v_max_u32 v7, v111, v7\n\t"
  "v_cmp_eq_u32 vcc, -1, v7\n\t"
  "s_cbranch_vccz LS35_%=\n\t"
  "buffer_load_dwordx4 v[104:107], v0, s[36:39], s55 offen offset:2048 sc1\n\t"
  "buffer_load_dwordx4 v[108:111], v0, s[36:39], s55 offen offset:3072 sc1\n\t"
  "LS35_%=:\n\t"
  "v_max3_u32 v7, v113, v115, v117\n\t"
  "v_max_u32 v7, v119, v7\n\t"
  "v_cmp_eq_u32 vcc, -1, v7\n\t"
  "s_cbranch_vccz LS36_%=\n\t"
  "buffer_load_dwordx4 v[112:115], v0, s[36:39], s56 offen offset:0 sc1\n\t"
  "buffer_load_dwordx4 v[116:119], v0, s[36:39], s56 offen offset:1024 sc1\n\t"
  "LS36_%=:\n\t"
  "v_max3_u32 v7, v121, v123, v125\n\t"
  "v_max_u32 v7, v127, v7\n\t"
  "v_cmp_eq_u32 vcc, -1, v7\n\t"
  "s_cbranch_vccz LS37_%=\n\t"
  "buffer_load_dwordx4 v[120:123], v0, s[36:39], s56 offen offset:2048 sc1\n\t"
  "buffer_load_dwordx4 v[124:127], v0, s[36:39], s56 offen offset:3072 sc1\n\t"
  "LS37_%=:\n\t"
  "s_waitcnt vmcnt(0)\n\t"
  "s_branch LC3_%=\n\t"
  "LR4_%=:\n\t"
  "s_sub_u32 s53, s53, 1\n\t"
  "s_cbranch_scc1 LD4_%=\n\t"
  "s_waitcnt vmcnt(0)\n\t"
  "buffer_load_dwordx4 v[96:99], v0, s[36:39], s55 offen offset:0 sc1\n\t"
  "buffer_load_dwordx4 v[100:103], v0, s[36:39], s55 offen offset:1024 sc1\n\t"
  "v_max3_u32 v7, v105, v107, v109\n\t"
  "v_max_u32 v7, v111, v7\n\t"
  "v_cmp_eq_u32 vcc, -1, v7\n\t"
  "s_cbranch_vccz LS45_%=\n\t"
  "buffer_load_dwordx4 v[104:107], v0, s[36:39], s55 offen offset:2048 sc1\n\t"
  "buffer_load_dwordx4 v[108:111], v0, s[36:39], s55 offen offset:3072 sc1\n\t"
  "LS45_%=:\n\t"
  "v_max3_u32 v7, v113, v115, v117\n\t"
  "v_max_u32 v7, v119, v7\n\t"
  "v_cmp_eq_u32 vcc, -1, v7\n\t"
  "s_cbranch_vccz LS46_%=\n\t"
  "buffer_load_dwordx4 v[112:115], v0, s[36:39], s56 offen offset:0 sc1\n\t"
  "buffer_load_dwordx4 v[116:119], v0, s[36:39], s56 offen offset:1024 sc1\n\t"
  "LS46_%=:\n\t"
  "v_max3_u32 v7, v121, v123, v125\n\t"
  "v_max_u32 v7, v127, v7\n\t"
  "v_cmp_eq_u32 vcc, -1, v7\n\t"
  "s_cbranch_vccz LS47_%=\n\t"
  "buffer_load_dwordx4 v[120:123], v0, s[36:39], s56 offen offset:2048 sc1\n\t"
  "buffer_load_dwordx4 v[124:127], v0, s[36:39], s56 offen offset:3072 sc1\n\t"
  "LS47_%=:\n\t"
  "s_waitcnt vmcnt(0)\n\t"
  "s_branch LC4_%=\n\t"
  "LR5_%=:\n\t"
  "s_sub_u32 s53, s53, 1\n\t"
  "s_cbranch_scc1 LD5_%=\n\t"
  "s_waitcnt vmcnt(0)\n\t"
  "buffer_load_dwordx4 v[104:107], v0, s[36:39], s55 offen offset:2048 sc1\n\t"
  "buffer_load_dwordx4 v[108:111], v0, s[36:39], s55 offen offset:3072 sc1\n\t"
  "v_max3_u32 v7, v113, v115, v117\n\t"
  "v_max_u32 v7, v119, v7\n\t"
  "v_cmp_eq_u32 vcc, -1, v7\n\t"
  "s_cbranch_vccz LS56_%=\n\t"
  "buffer_load_dwordx4 v[112:115], v0, s[36:39], s56 offen offset:0 sc1\n\t"
  "buffer_load_dwordx4 v[116:119], v0, s[36:39], s56 offen offset:1024 sc1\n\t"
  "LS56_%=:\n\t"
  "v_max3_u32 v7, v121, v123, v125\n\t"
  "v_max_u32 v7, v127, v7\n\t"
  "v_cmp_eq_u32 vcc, -1, v7\n\t"
  "s_cbranch_vccz LS57_%=\n\t"
  "buffer_load_dwordx4 v[120:123], v0, s[36:39], s56 offen offset:2048 sc1\n\t"
  "buffer_load_dwordx4 v[124:127], v0, s[36:39], s56 offen offset:3072 sc1\n\t"
  "LS57_%=:\n\t"
  "s_waitcnt vmcnt(0)\n\t"
  "s_branch LC5_%=\n\t"
  "LR6_%=:\n\t"
  "s_sub_u32 s53, s53, 1\n\t"
  "s_cbranch_scc1 LD6_%=\n\t"
  "s_waitcnt vmcnt(0)\n\t"
  "buffer_load_dwordx4 v[112:115], v0, s[36:39], s56 offen offset:0 sc1\n\t"
  "buffer_load_dwordx4 v[116:119], v0, s[36:39], s56 offen offset:1024 sc1\n\t"
  "v_max3_u32 v7, v121, v123, v125\n\t"
  "v_max_u32 v7, v127, v7\n\t"
  "v_cmp_eq_u32 vcc, -1, v7\n\t"
  "s_cbranch_vccz LS67_%=\n\t"
  "buffer_load_dwordx4 v[120:123], v0, s[36:39], s56 offen offset:2048 sc1\n\t"
  "buffer_load_dwordx4 v[124:127], v0, s[36:39], s56 offen offset:3072 sc1\n\t"
  "LS67_%=:\n\t"
  "s_waitcnt vmcnt(0)\n\t"
  "s_branch LC6_%=\n\t"
  "LR7_%=:\n\t"
  "s_sub_u32 s53, s53, 1\n\t"
  "s_cbranch_scc1 LD7_%=\n\t"
  "s_waitcnt vmcnt(0)\n\t"
  "buffer_load_dwordx4 v[120:123], v0, s[36:39], s56 offen offset:2048 sc1\n\t"
  "buffer_load_dwordx4 v[124:127], v0, s[36:39], s56 offen offset:3072 sc1\n\t"
  "s_waitcnt vmcnt(0)\n\t"
  "s_branch LC7_%=\n\t"
  "LE_%=:\n\t"
  "s_nop 1\n\t"
        : "+{v[0:7]}"(ta)
        : "{s[36:39]}"(rw), "{s72}"(ol), "{s73}"(oc), "{s74}"(rr), "{s[76:79]}"(rout)
        : "v8", "v9", "v12", "v13", "v32", "v33", "v34", "v35", "v36", "v37", "v38", "v39", "v40", "v41", "v42", "v43", "v44", "v45", "v46", "v47", "v48", "v49", "v50", "v51", "v52", "v53", "v54", "v55", "v56", "v57", "v58", "v59", "v60", "v61", "v62", "v63", "v64", "v65", "v66", "v67", "v68", "v69", "v70", "v71", "v72", "v73", "v74", "v75", "v76", "v77", "v78", "v79", "v80", "v81", "v82", "v83", "v84", "v85", "v86", "v87", "v88", "v89", "v90", "v91", "v92", "v93", "v94", "v95", "v96", "v97", "v98", "v99", "v100", "v101", "v102", "v103", "v104", "v105", "v106", "v107", "v108", "v109", "v110", "v111", "v112", "v113", "v114", "v115", "v116", "v117", "v118", "v119", "v120", "v121", "v122", "v123", "v124", "v125", "v126", "v127", "v128", "v129", "v130", "v131", "v132", "v133", "v134", "v135", "v136", "v137", "v138", "v139", "v140", "v141", "v142", "v143", "v144", "v145", "v146", "v147", "v148", "v149", "v150", "v151", "v152", "v153", "v154", "v155", "v156", "v157", "v158", "v159", "v160", "v161", "v162", "v163", "v164", "v165", "v166", "v167", "v168", "v169", "v170", "v171", "v172", "v173", "v174", "v175", "v176", "v177", "v178", "v179", "v180", "v181", "v182", "v183", "v184", "v185", "v186", "v187", "v188", "v189", "v190", "v191", "v192", "v193", "v194", "v195", "v196", "v197", "v198", "v199", "v200", "v201", "v202", "v203", "v204", "v205", "v206", "v207", "v208", "v209", "v210", "v211", "v212", "v213", "v214", "v215", "v216", "v217", "v218", "v219", "v220", "v221", "v222", "v223", "v224", "v225", "v226", "v227", "v228", "v229", "v230", "v231", "v232", "v233", "v234", "v235", "v236", "v237", "v238", "v239", "v240", "v241", "v242", "v243", "v244", "v245", "v246", "v247", "v248", "v249", "v250", "v251", "v252", "v253", "v254", "v255", "s52", "s53", "s54", "s55", "s56", "vcc", "scc", "memory"
    );
}

DI u64 load_and_b1(const Params& p, lds_p lds, int x, int r, int lw, int lane) {
  const int g16 = wg_h(x, r), t = wg_t(x, r), n2 = 8 * x + (r >> 2), jsl = r & 3;
  int cc = wg_c0(r);
  rsrc_t rx = mkrsrc(p.xa, SEQ * DM * 2);
  const int l3 = lane >> 3;
  const unsigned vx0 = (128 * t + 32 * lw + l3) * 896 + (((lane & 7) ^ (lane >> 4)) << 4), vx1 = (vx0 ^ 64) + 8 * 896;
#define X_LD(i) do { const unsigned sl_ = ((i) % 5) * SLOT + lw * 4096, sx_ = cc * 128; \
    dma16(sl_, vx0, rx, sx_); dma16(sl_ + 1024, vx1, rx, sx_); dma16(sl_ + 2048, vx0, rx, sx_ + 16 * 896); dma16(sl_ + 3072, vx1, rx, sx_ + 16 * 896); } while (0)
#define W_LD(i) do { const unsigned sq_ = cc * (64 * 2688), sw_ = ((i) % 5) * SLOT + 16384 + lw * 2048; cc = cc == 6 ? 0 : cc + 1; \
    dma16(sw_, vw, rq, sq_); dma16(sw_ + 1024, vw, rq, sq_ + 8 * 2688); } while (0)
#define KV_LD(i) do { X_LD(i); W_LD(i); } while (0)
  X_LD(0);
  SBAR();
  rsrc_t rq = mkrsrc(p.wqkv, DM * 3 * DM * 2), rb = mkrsrc(p.xb, SEQ * DM * 2), rt = mkrsrc(&g_tab, TB_BYTES);
  const unsigned vw = (16 * lw + l3) * 2688 + 896 + wg_g(x, r) * 112 + (((lane & 7) ^ (((lane >> 4) & 1) << 2)) << 4);
  W_LD(0);
  KV_LD(1);
  KV_LD(2);
  const unsigned vq = (16 * lw + (lane >> 2)) * 2688 + g16 * 56 + (lane & 3) * 16;
  BAR_V(12);
  KV_LD(3); BAR_V(12);
  KV_LD(4); BAR_V(12);
  KV_LD(5); BAR_V(12);
  KV_LD(6); BAR_V(12);
#undef KV_LD
#undef X_LD
#undef W_LD
  u32x2 ep;
  asm volatile("buffer_load_dwordx2 %0, %1, %2, 0 offen sc1" : "=v"(ep) : "v"(0u), "s"(mkrsrc(p.ws, 8)) : "memory");
#define WQ_LD(i) do { dma16(LDS_WQ + (i) * 4096 + lw * 1024, vq, rq, cc * (64 * 2688)); cc = cc == 6 ? 0 : cc + 1; } while (0)
  WQ_LD(0); WQ_LD(1); WQ_LD(2);
  BAR_V(10);
  WQ_LD(3); WQ_LD(4);
  asm volatile("s_waitcnt vmcnt(5)\n\ts_barrier" : "+v"(ep) : : "memory");
  const u64 tag = mktag(((u64)__builtin_amdgcn_readfirstlane(ep[1]) << 32) | (unsigned)__builtin_amdgcn_readfirstlane(ep[0]));
  *(LAS u64*)(lds + LDS_CNT + 4 * C_TAG) = tag;
  BAR_L();
  WQ_LD(5); WQ_LD(6);
#undef WQ_LD
  u32x4 xin[7], F[14], E[4];
  const int rl = 8 * lw + l3, v = 4 * jsl + lw, part = v & 1, c16 = lane & 15, g = lane >> 4;
#pragma unroll
  for (int c = 0; c < 7; ++c) aload(xin[c], (64 * rl + n2) * 896 + (lane & 7) * 16, rb, c * 128);
  WAITV(7); lds_arrive(C_WQ);
  if (__builtin_expect(blockIdx.x == 24 && lw == 3, 0)) { set_flag(p.ws, F_CNT + (lane & 7) * 256, tag); WAITV(0); set_flag(p.ws, F_INIT, tag); }
  if (v < 14) {
#pragma unroll
    for (int i = 0; i < 14; ++i) aload(F[i], lane * 16, rt, TB_F1 + v * 14336 + i * 1024);
#pragma unroll
    for (int i = 0; i < 4; ++i) aload(E[i], lane * 16, rt, TB_E1 + n2 * 4096 + ((i ^ part) * 1024));
  }
  __builtin_amdgcn_s_setprio(2);
  if (v < 14) WAITV(18); else WAITV(0);
  lds_p img = lds + LDS_XIMG;
  { lds_p a = img + rl * 480 + (lane & 7) * 8;
#pragma unroll
    for (int c = 0; c < 7; ++c) { PINV(xin[c]);
      *(LAS u32x2*)(a + c * 64) = (u32x2){__builtin_amdgcn_perm(xin[c][1], xin[c][0], 0x05040100u), __builtin_amdgcn_perm(xin[c][3], xin[c][2], 0x05040100u)};
      *(LAS u32x2*)(a + c * 64 + 15360) = (u32x2){__builtin_amdgcn_perm(xin[c][1], xin[c][0], 0x07060302u), __builtin_amdgcn_perm(xin[c][3], xin[c][2], 0x07060302u)}; } }
  role_bar(C_BBAR, 4);
  u64 ini = 0;
  if (v < 14) {
    lds_p xa = img + c16 * 480 + g * 16;
    f32x4 e0 = {}, e1 = {}, o0 = {}, o1 = {};
    u32x4 xf[28];
    WAITV(0);
#pragma unroll
    for (int i = 0; i < 14; ++i) PINV(F[i]);
#pragma unroll
    for (int i = 0; i < 4; ++i) PINV(E[i]);
    ini = __hip_atomic_load((GAS u64*)(p.ws + F_INIT), __ATOMIC_RELAXED, __HIP_MEMORY_SCOPE_AGENT);
    SBAR();
#pragma unroll
    for (int c = 0; c < 28; ++c) xf[c] = ldsr128(xa + (c >> 2) * 64 + (c & 3) * 7680);
#pragma unroll
    for (int ks = 0; ks < 7; ++ks) {
      e0 = MFMA16(asfrag(xf[4 * ks]), asfrag(F[ks]), e0); e1 = MFMA16(asfrag(xf[4 * ks + 1]), asfrag(F[ks]), e1);
      o0 = MFMA16(asfrag(xf[4 * ks + 2]), asfrag(F[7 + ks]), o0); o1 = MFMA16(asfrag(xf[4 * ks + 3]), asfrag(F[7 + ks]), o1);
    }
    u32x4 X = pk44(e0 + o0, e1 + o1), give = pk44(e0 - o0, e1 - o1);
    if (__builtin_expect(v == 1, 0)) { const u32x4 od = pk44(o0, o1); X = c16 ? X : od; give = c16 ? give : (u32x4){0, 0, 0, 0}; }
    ldsw128(lds + LDS_XCH + lw * 1024 + lane * 16, give);
    role_bar(C_X1 + (lw >> 1), 2);
    u32x4 Y = ldsr128(lds + LDS_XCH + (lw ^ 1) * 1024 + lane * 16);
    if (__builtin_expect(v == 1, 0)) { const u32x4 ev = pk44(e0, e1); Y = c16 ? Y : ev; }
    const u32x4 Xs = xor4(X, part ? 0x80008000u : 0u), Ys = xor4(Y, part ? 0u : 0x80008000u);
    rsrc_t rw = mkrsrc(p.ws, WS_END);
    const unsigned tv = c16 * (15 * 4096) + g * 16;
#pragma unroll
    for (int kt = 0; kt < 2; ++kt) {
      f32x4 tr = {}, ti = {};
      tr = MFMA16(asfrag(Xs), asfrag(E[2 * kt]), tr); ti = MFMA16(asfrag(X), asfrag(E[2 * kt + 1]), ti);
      tr = MFMA16(asfrag(Y), asfrag(E[2 * kt + 1]), tr); ti = MFMA16(asfrag(Ys), asfrag(E[2 * kt]), ti);
      ST128(pk44(tr, ti), rw, tv, WS_T + kt * (16 * 15 * 4096) + v * 4096 + n2 * 64, SC1);
    }
    if (__builtin_expect(v == 0, 0)) {
#pragma unroll
      for (int kt = 0; kt < 2; ++kt) { f32x4 nr = {}, ni = {};
        nr = MFMA16(asfrag(give), asfrag(E[2 * kt]), nr); ni = MFMA16(asfrag(give), asfrag(E[2 * kt + 1]), ni);
        ST128(pk44(nr, ni), rw, tv, WS_T + kt * (16 * 15 * 4096) + 14 * 4096 + n2 * 64, SC1); }
    }
  }
  {
    rsrc_t rw = mkrsrc(p.ws, WS_END); const unsigned oo = WS_O + ((4 * t + lw) * 16 + g16) * 2048; const u32x4 ones = {~0u, ~0u, ~0u, ~0u};
    ST128(ones, rw, lane * 16, oo, SC1); ST128(ones, rw, lane * 16 + 1024, oo, SC1); }
  if (drain_last(C_TD, 4)) {
    if (!__all(ini == tag)) wait_flags<4>(p.ws, F_INIT, tag);
    count_and_tell(p.ws, jsl, 64, tag, lane); }
  return tag;
}

template <int C0, int C1> DI void load_wo(const void* wo, int jc, int lw, int lane) {
  rsrc_t ro = mkrsrc(wo, DM * DM * 2);
  const unsigned vo = (112 * lw + (lane >> 3)) * 896 + jc * 128 + (((lane & 7) ^ (((lane >> 4) & 1) << 2)) << 4);
#pragma unroll
  for (int c = C0; c < C1; ++c) dma16(LDS_WO + lw * 14336 + c * 1024, vo, ro, c * 8 * 896);
}

DI void b2(const Params& p, lds_p lds, u64 tag, int k1, int ft, int lw, int nw, int lane) {
  const int c16 = lane & 15, g = lane >> 4, hf = lw & 1;
  rsrc_t rt = mkrsrc(&g_tab, TB_BYTES), rw = mkrsrc(p.ws, WS_END), rr = mkrsrc(p.wr, SEQ * 225 * 2), ri = mkrsrc(p.wi, SEQ * 225 * 2);
  u32x4 tin[4], C[12]; float wr[8], wi[8];
  lds_wait<4>(C_TD, 4); lds_wait<4>(C_QDONE, 4);
  lds_p xc = lds + LDS_BIMG + 16384;
  {
    const bool up = !(ft & 1) && ft < 14;
    const unsigned wo = ((k1 + 32 * (32 * hf + (lane >> 1))) * 225 + (up ? 8 * ft : 209 - 8 * (ft & ~1) + (ft == 14 ? 112 : 0)) + 8 * (lane & 1)) * 2;
    const u32x4 a = LD128(rr, wo, 0, 0), b = LD128(ri, wo, 0, 0);
    ldsw128(xc + lw * 2048 + lane * 16, a); ldsw128(xc + lw * 2048 + 1024 + lane * 16, b);
    if (__builtin_expect(ft == 1, 0)) {
      const unsigned so = ((k1 + 32 * (32 * hf + (lane & 31))) * 225 + 112) * 2;
      const unsigned short u = __builtin_amdgcn_raw_buffer_load_b16(rr, so, 0, 0), v = __builtin_amdgcn_raw_buffer_load_b16(ri, so, 0, 0);
      *(LAS unsigned short*)(xc + lw * 2048 + (lane & 31) * 32 + 30) = u; *(LAS unsigned short*)(xc + lw * 2048 + 1024 + (lane & 31) * 32 + 30) = v; }
    const unsigned wa = LDS_BIMG + 16384 + lw * 2048 + g * 128 + (up ? c16 : ft == 14 ? 15 : 15 - c16) * 2;
#define WRD(e) "ds_read_u16_d16_hi %" #e ", %8 offset:%c9+"
    asm volatile(WRD(0) "0\n\t" WRD(1) "32\n\t" WRD(2) "64\n\t" WRD(3) "96\n\t" WRD(4) "512\n\t" WRD(5) "544\n\t" WRD(6) "576\n\t" WRD(7) "608\n\ts_waitcnt lgkmcnt(0)"
                 : "=&v"(wr[0]), "=&v"(wr[1]), "=&v"(wr[2]), "=&v"(wr[3]), "=&v"(wr[4]), "=&v"(wr[5]), "=&v"(wr[6]), "=&v"(wr[7]) : "v"(wa), "i"(0) : "memory");
    asm volatile(WRD(0) "0\n\t" WRD(1) "32\n\t" WRD(2) "64\n\t" WRD(3) "96\n\t" WRD(4) "512\n\t" WRD(5) "544\n\t" WRD(6) "576\n\t" WRD(7) "608\n\ts_waitcnt lgkmcnt(0)"
                 : "=&v"(wi[0]), "=&v"(wi[1]), "=&v"(wi[2]), "=&v"(wi[3]), "=&v"(wi[4]), "=&v"(wi[5]), "=&v"(wi[6]), "=&v"(wi[7]) : "v"(wa), "i"(1024) : "memory");
#undef WRD
  }
#pragma unroll
  for (int i = 0; i < 12; ++i) aload(C[i], lane * 16, rt, TB_F64 + hf * 12288 + (i / 6) * 6144 + (i % 3) * 1024 + ((((i / 3) & 1) ^ hf) * 3072));
  if (lw == 0) { wait_flags<GSL>(p.ws, F_B + (blockIdx.x & (NREP - 1)) * 128 + (lane & 3) * 8, tag); lds_arrive(C_FT); lds_arrive(C_MARK); } else lds_wait<SLP>(C_FT, 1);
#pragma unroll
  for (int i = 0; i < 4; ++i) tin[i] = LD128(rw, lane * 16, WS_T + (k1 * 15 + ft) * 4096 + i * 1024, SC1);
  lds_p my = lds + LDS_BIMG + lw * 4096;
#pragma unroll
  for (int i = 0; i < 4; ++i) ldsw128(my + i * 1024 + lane * 16, tin[i]);
  WAITV(0);
#pragma unroll
  for (int i = 0; i < 12; ++i) PINV(C[i]);
  bf16x8 Tr[2], Ti[2];
  lds_p ta = my + (4 * g + ((lane >> 2) & 3)) * 64 + (lane & 3) * 16;
#pragma unroll
  for (int s = 0; s < 2; ++s) { lds_p tb_ = ta + (s ^ hf) * 2048; Tr[s] = cat(ldstr(tb_), ldstr(tb_ + 1024)); Ti[s] = cat(ldstr(tb_ + 8), ldstr(tb_ + 1024 + 8)); }
  f32x4 Xr[2], Xi[2];
#pragma unroll
  for (int t = 0; t < 2; ++t) { f32x4 a = {}, b = {};
#pragma unroll
    for (int s = 0; s < 2; ++s) { const bf16x8 cr = asfrag(C[6 * t + 3 * s]), ci = asfrag(C[6 * t + 3 * s + 1]), cn = asfrag(C[6 * t + 3 * s + 2]);
      a = MFMA16(cr, Tr[s], a); b = MFMA16(ci, Tr[s], b); a = MFMA16(cn, Ti[s], a); b = MFMA16(cr, Ti[s], b); }
#pragma unroll
    for (int r = 0; r < 4; ++r) { const float c = wr[4 * t + r], d = wi[4 * t + r]; Xr[t][r] = a[r] * c - b[r] * d; Xi[t][r] = a[r] * d + b[r] * c; } }
  const u32x4 yr = pk44(Xr[0], Xr[1]), yi = pk44(Xi[0], Xi[1]);
  xc += lane * 16;
  ldsw128(xc + lw * 2048, yr); ldsw128(xc + lw * 2048 + 1024, yi);
  role_bar(C_X2 + (lw >> 1), 2);
  const u32x4 zr = ldsr128(xc + (lw ^ 1) * 2048), zi = ldsr128(xc + (lw ^ 1) * 2048 + 1024);
  const unsigned uo = WS_U + hf * 32 * 32768 + (ft >> 1) * 4096 + k1 * 128 + (ft & 1) * 64;
#pragma unroll
  for (int t = 0; t < 2; ++t) { f32x4 a = {}, b = {};
    a = MFMA16(asfrag(yr), asfrag(C[6 * t]), a);     b = MFMA16(asfrag(yi), asfrag(C[6 * t]), b);
    a = MFMA16(asfrag(yi), asfrag(C[6 * t + 1]), a); b = MFMA16(asfrag(yr), asfrag(C[6 * t + 2]), b);
    a = MFMA16(asfrag(zr), asfrag(C[6 * t + 3]), a); b = MFMA16(asfrag(zi), asfrag(C[6 * t + 3]), b);
    a = MFMA16(asfrag(zi), asfrag(C[6 * t + 4]), a); b = MFMA16(asfrag(zr), asfrag(C[6 * t + 5]), b);
    ST128(pk44(a, b), rw, c16 * 32768 + g * 16, uo + t * 16 * 32768, SC1); }
  if (drain_last(C_UD, nw)) count_and_tell(p.ws, 4 + (blockIdx.x & 3), 64, tag, lane);
}

DI void b3(const Params& p, lds_p lds, u64 tag, int n2, int jsl, int jc, int lw, int lane) {
  const int c16 = lane & 15, g = lane >> 4, v = 4 * jsl + lw;
  rsrc_t rw = mkrsrc(p.ws, WS_END);
  lds_p ub = lds + LDS_BIMG;
  u32x4 E[4], G[14], uin[8];
  rsrc_t rt = mkrsrc(&g_tab, TB_BYTES);
#pragma unroll
  for (int c = 0; c < 4; ++c) E[c] = LD128(rt, lane * 16, TB_E2 + n2 * 4096 + c * 1024, 0);
#pragma unroll
  for (int c = 0; c < 14; ++c) { G[c] = LD128(rt, v < 14 ? lane * 16 : OOB, TB_G1 + v * 14336 + c * 1024, 0); if (c == 4) WAITV(0); }
  if (jc >= 0) { WAITV(0); load_wo<0, 10>(p.wo, jc, lw, lane); }
  if (lw == 3) { wait_flags<GSL>(p.ws, F_B + (blockIdx.x & (NREP - 1)) * 128 + 32 + (lane & 3) * 8, tag); lds_arrive(C_FU); } else lds_wait<SLP>(C_FU, 1);
#pragma unroll
  for (int c = 0; c < 8; ++c) uin[c] = LD128(rw, lane * 16, WS_U + n2 * 32768 + (lw + 4 * (c >> 2)) * 4096 + (c & 3) * 1024, SC1);
  const int wsw = (lane >> 3) * 128 + ((((lane >> 2) ^ (lane >> 4)) & 1) << 6) + (lane & 3) * 16;
  lds_p u0 = ub + lw * 4096 + wsw, u1 = (lw == 3 ? lds + LDS_NYQ : ub + (lw + 4) * 4096) + wsw;
#pragma unroll
  for (int c = 0; c < 4; ++c) { ldsw128(u0 + c * 1024, uin[c]); ldsw128(u1 + c * 1024, uin[4 + c]); }
  if (lw == 3) lds_arrive(C_NYQ);
  if (jc >= 0) { load_wo<10, 14>(p.wo, jc, lw, lane); WAITV(0); lds_arrive(C_WO); }
  const u32x4 En[2] = {xor4(E[1], 0x80008000u), xor4(E[3], 0x80008000u)};
  const int q = (lane >> 2) & 3;
  lds_p ta = ub + lw * 4096 + (4 * g + q) * 128 + (lane & 3) * 16;
  lds_p tlo = ta + ((q >> 1) << 6), thi = ta + (((q >> 1) ^ 1) << 6);
  bf16x8 U[2][4], N[2];
  __builtin_amdgcn_s_setprio(0);
#pragma unroll
  for (int b = 0; b < 2; ++b)
#pragma unroll
    for (int k = 0; k < 4; ++k) { lds_p a = ((k >> 1) ? thi : tlo) + (b && lw < 3 ? 16384 : 0) + (k & 1) * 8; U[b][k] = cat(ldstr(a), ldstr(a + 2048)); }
  if (lw == 0) lds_wait(C_NYQ, 1);
  { lds_p tn = lds + LDS_NYQ + (4 * g + q) * 128 + (lane & 3) * 16 + ((q >> 1) << 6); N[0] = cat(ldstr(tn), ldstr(tn + 2048)); N[1] = cat(ldstr(tn + 8), ldstr(tn + 2048 + 8)); }
#pragma unroll
  for (int k = 0; k < 8; ++k) PINV(U[k >> 2][k & 3]);
  PINV(N[0]); PINV(N[1]);
  lds_wait<2>(C_RED, 4);
  f32x4 z[2][2][4], zn[2];
#pragma unroll
  for (int nt = 0; nt < 2; ++nt) { const bf16x8 er = asfrag(E[2 * nt]), ei = asfrag(E[2 * nt + 1]), en = asfrag(En[nt]); const f32x4 o = {};
#pragma unroll
    for (int b = 0; b < 2; ++b) {
      z[b][nt][0] = MFMA16(U[b][0], er, o); z[b][nt][1] = MFMA16(U[b][0], ei, o); z[b][nt][2] = MFMA16(U[b][2], er, o); z[b][nt][3] = MFMA16(U[b][2], ei, o); }
    zn[nt] = MFMA16(N[0], er, o);
#pragma unroll
    for (int b = 0; b < 2; ++b) {
      z[b][nt][0] = MFMA16(U[b][1], en, z[b][nt][0]); z[b][nt][1] = MFMA16(U[b][1], er, z[b][nt][1]); z[b][nt][2] = MFMA16(U[b][3], en, z[b][nt][2]); z[b][nt][3] = MFMA16(U[b][3], er, z[b][nt][3]); }
    zn[nt] = MFMA16(N[1], en, zn[nt]);
  }
  const bool spl = (lw | g) == 0;
  lds_p zw = ub + lw * 4096 + lane * 16;
#pragma unroll
  for (int nt = 0; nt < 2; ++nt)
#pragma unroll
    for (int b = 0; b < 2; ++b) { const f32x4 lr = z[b][nt][0], li = z[b][nt][1], hr = z[b][nt][2], hi = z[b][nt][3];
      f32x4 ae = lr + hr, be = li - hi, ao = lr - hr, bo = li + hi;
      if (b == 0) { ae[0] = spl ? lr[0] + zn[nt][0] : ae[0]; ao[0] = spl ? lr[0] - zn[nt][0] : ao[0]; be[0] = spl ? hr[0] : be[0]; bo[0] = spl ? hi[0] : bo[0]; }
      if (b == 0 || lw < 3) { ldsw128(zw + b * 16384 + nt * 1024, pk44(ae, be)); ldsw128(zw + b * 16384 + 2048 + nt * 1024, pk44(ao, bo)); } }
  role_bar(C_BBAR, 8);
  if (blockIdx.x == 0 && lw == 0 && lane == 0) set_flag(p.ws, F_EP, tag);
  if (v < 14) {
    f32x4 y[2][2] = {}; u32x4 zf[28];
#pragma unroll
    for (int c = 0; c < 28; ++c) zf[c] = ldsr128(ub + c * 1024 + lane * 16);
    SBAR();
#pragma unroll
    for (int c = 0; c < 28; ++c) y[c & 1][(c >> 1) & 1] = MFMA16(asfrag(G[c >> 1]), asfrag(zf[c]), y[c & 1][(c >> 1) & 1]);
    rsrc_t rout = mkrsrc(p.out, SEQ * 896 * 2);
#pragma unroll
    for (int nt = 0; nt < 2; ++nt)
      ST128(((u32x4){cvtpk(y[nt][0][0], y[nt][1][0]), cvtpk(y[nt][0][1], y[nt][1][1]), cvtpk(y[nt][0][2], y[nt][1][2]), cvtpk(y[nt][0][3], y[nt][1][3])}),
            rout, c16 * (64 * 1792) + g * 16, (nt * 1024 + n2) * 1792 + 896 + v * 64, 0);
  }
}

__global__ __launch_bounds__(512, 2) void mha_gfnet_fused(Params p) {
  extern __shared__ __attribute__((aligned(16))) char smem[];
  lds_p lds = (lds_p)smem;
  const int lane = threadIdx.x & 63, wid = __builtin_amdgcn_readfirstlane(threadIdx.x >> 6);
  const int x = blockIdx.x & 7, r = blockIdx.x >> 3;
  asm volatile("" ::"s"(p.ws), "s"(p.xa), "s"(p.wqkv), "s"(p.wo), "s"(p.xb), "s"(p.wr), "s"(p.wi), "s"(p.out));
  if (wid < 4) {
    if (wid == 0) ((LAS unsigned*)(lds + LDS_CNT))[lane & 31] = 0;
    role_a(p, lds, x, r, wid, lane);
  } else {
    const int lw = wid - 4;
    const u64 tag = load_and_b1(p, lds, x, r, lw, lane);
    if (lw < (r < 28 ? 4 : 2)) b2(p, lds, tag, 4 * x + (r < 28 ? r / 7 : r - 28), r < 28 ? 2 * (r % 7) + (lw >> 1) : 14, lw, r < 28 ? 4 : 2, lane);
    b3(p, lds, tag, 8 * x + (r >> 2), r & 3, r < 28 ? r % 7 : -1, lw, lane);
  }
}

extern "C" void kernel_launch(void* const* d_in, const int* in_sizes, int n_in, void* d_out, int out_size, void* d_ws, size_t ws_size, hipStream_t stream) {
  Params p; p.xa = d_in[0]; p.wqkv = d_in[1]; p.wo = d_in[2]; p.xb = d_in[3]; p.wr = d_in[4]; p.wi = d_in[5]; p.out = d_out; p.ws = (char*)d_ws;
  (void)hipFuncSetAttribute((const void*)mha_gfnet_fused, hipFuncAttributeMaxDynamicSharedMemorySize, LDS_BYTES);
  mha_gfnet_fused<<<256, 512, LDS_BYTES, stream>>>(p);
}
```
